```python
import torch

def _softmax(x, axis=-1):
    return torch.softmax(x, dim=axis)
def _gelu(x):
    return torch.nn.functional.gelu(x, approximate="tanh")
def _silu(x):
    return torch.nn.functional.silu(x)
def _sigmoid(x):
    return torch.sigmoid(x)
def _layernorm(x, axis=-1, eps=1e-5):
    m = x.mean(dim=axis, keepdim=True)
    v = x.var(dim=axis, keepdim=True, unbiased=False)
    return (x - m) / torch.sqrt(v + eps)
def _scatter_add(tgt, idx, src, axis=0):
    return tgt.clone().index_add_(axis, idx, src)
def _segment_sum(src, seg, n):
    out = torch.zeros((n,) + src.shape[1:], dtype=src.dtype)
    return out.index_add_(0, seg, src)
def _pad(x, pw):
    flat = tuple(v for pair in reversed(pw) for v in pair)
    return torch.nn.functional.pad(x, flat)

kernel_name = 'sequential_preLN__linear_attn_block__geglu_ffn__prompt'

def setup_inputs():

    _w = (lambda a: (lambda t: t.to(torch.bfloat16) if t.is_floating_point() else t)(torch.as_tensor(a)))
    def _A_setup():
        B, S, D = 1, 2048, 1024
        return {
            "x": _w((lambda *s: torch.randn(*s, dtype=torch.bfloat16) * 0.05)(B, S, D)),
            "wqkv": _w((lambda *s: torch.randn(*s, dtype=torch.bfloat16) * 0.05)(D, 3 * D)),
            "wo": _w((lambda *s: torch.randn(*s, dtype=torch.bfloat16) * 0.05)(D, D)),
        }
    _A_in = _A_setup()

    def _B_setup():
        B, S, D, F = 1, 2048, 1024, 3584
        return {
            "x": _w((lambda *s: torch.randn(*s, dtype=torch.bfloat16) * 0.05)(B, S, D)),
            "w_gate": _w((lambda *s: torch.randn(*s, dtype=torch.bfloat16) * 0.05)(D, F)),
            "w_up": _w((lambda *s: torch.randn(*s, dtype=torch.bfloat16) * 0.05)(D, F)),
            "w_down": _w((lambda *s: torch.randn(*s, dtype=torch.bfloat16) * 0.05)(F, D)),
        }
    _B_in = _B_setup()

    return {
        "A_x": _A_in["x"],
        "A_wqkv": _A_in["wqkv"],
        "A_wo": _A_in["wo"],
        "B_x": _B_in["x"],
        "B_w_gate": _B_in["w_gate"],
        "B_w_up": _B_in["w_up"],
        "B_w_down": _B_in["w_down"],
    }

def reference(A_x, A_wqkv, A_wo, B_x, B_w_gate, B_w_up, B_w_down):
    def _A_ref(x, wqkv, wo):
        B, S, D = x.shape
        H = 32; Dh = D // H
        qkv = (lambda x, *d: x.permute(*d))((x @ wqkv).reshape(B, S, 3, H, Dh), 2, 0, 3, 1, 4)
        q, k, v = qkv[0], qkv[1], qkv[2]
        phi = lambda t: torch.where(t > 0, t + 1, torch.exp(t)) + 1e-6
        pq, pk = phi(q), phi(k)
        kv = torch.einsum("bhsd,bhse->bhde", pk, v)
        z = torch.einsum("bhsd->bhd", pk)
        num = torch.einsum("bhqd,bhde->bhqe", pq, kv)
        den = torch.einsum("bhqd,bhd->bhq", pq, z)[..., None] + 1e-6
        o = num / den
        return (lambda x, *d: x.permute(*d))(o, 0, 2, 1, 3).reshape(B, S, D) @ wo

    def _B_ref(x, w_gate, w_up, w_down):
        return (_gelu(x @ w_gate) * (x @ w_up)) @ w_down

    x = A_x
    h = x + _A_ref(_layernorm(x, axis=-1), A_wqkv, A_wo)
    h2 = h + _B_ref(_layernorm(h, axis=-1), B_w_gate, B_w_up, B_w_down)
    return h2
```

```cpp
#include <hip/hip_runtime.h>
#include <hip/hip_bf16.h>
#include <stdint.h>

#define LAS __attribute__((address_space(3)))
typedef unsigned short u16;
typedef __bf16 bf16x4 __attribute__((ext_vector_type(4)));
typedef __bf16 bf16x8 __attribute__((ext_vector_type(8)));
typedef float f32x16 __attribute__((ext_vector_type(16)));
typedef float f32x4 __attribute__((ext_vector_type(4)));
typedef float f32x2 __attribute__((ext_vector_type(2)));
typedef unsigned u32x4 __attribute__((ext_vector_type(4)));
typedef unsigned u32x2 __attribute__((ext_vector_type(2)));
typedef __bf16 bf16x2 __attribute__((ext_vector_type(2)));

constexpr int S_ = 2048, D_ = 1024, F_ = 3584, H_ = 32;

__device__ __forceinline__ unsigned pack_bf16(float lo, float hi) {
  bf16x2 v; v[0] = (__bf16)lo; v[1] = (__bf16)hi;
  return __builtin_bit_cast(unsigned, v);
}
__device__ __forceinline__ float rbf(float x) { return (float)(__bf16)x; }
__device__ __forceinline__ float bf_lo(unsigned u) { return __builtin_bit_cast(float, u << 16); }
__device__ __forceinline__ float bf_hi(unsigned u) { return __builtin_bit_cast(float, u & 0xffff0000u); }

#define WAIT_V(n) asm volatile("s_waitcnt vmcnt(%0)" ::"i"(n) : "memory")
#define WAIT_L0() asm volatile("s_waitcnt lgkmcnt(0)" ::: "memory")
#define BAR() __builtin_amdgcn_s_barrier()

typedef __amdgpu_buffer_rsrc_t rsrc_t;
__device__ __forceinline__ rsrc_t mk_rsrc(const void* p, unsigned bytes) {
  return __builtin_amdgcn_make_buffer_rsrc((void*)p, (short)0, (int)bytes, 0x00020000);
}
__device__ __forceinline__ void glds16(rsrc_t r, LAS char* dst, unsigned voff, unsigned soff) {
  __builtin_amdgcn_raw_ptr_buffer_load_lds(r, (LAS void*)dst, 16, (int)voff, (int)soff, 0, 0);
}

__global__ __launch_bounds__(256) void ln_kernel(const u16* __restrict__ x, u16* __restrict__ y) {
  const int lane = threadIdx.x & 63, row = blockIdx.x * 4 + (threadIdx.x >> 6);
  const u32x4* xr = (const u32x4*)(x + (size_t)row * D_);
  u32x4 a = xr[lane], b = xr[lane + 64];
  float v[16];
#pragma unroll
  for (int i = 0; i < 4; ++i) { v[2 * i] = bf_lo(a[i]); v[2 * i + 1] = bf_hi(a[i]); v[8 + 2 * i] = bf_lo(b[i]); v[9 + 2 * i] = bf_hi(b[i]); }
  float s = 0.f;
#pragma unroll
  for (int i = 0; i < 16; ++i) s += v[i];
#pragma unroll
  for (int o = 32; o > 0; o >>= 1) s += __shfl_xor(s, o);
  const float m = s * (1.f / D_);
  float q = 0.f;
#pragma unroll
  for (int i = 0; i < 16; ++i) { v[i] -= m; q += v[i] * v[i]; }
#pragma unroll
  for (int o = 32; o > 0; o >>= 1) q += __shfl_xor(q, o);
  const float r = 1.f / sqrtf(q * (1.f / D_) + 1e-5f);
  u32x4 oa, ob;
#pragma unroll
  for (int i = 0; i < 4; ++i) { oa[i] = pack_bf16(v[2 * i] * r, v[2 * i + 1] * r); ob[i] = pack_bf16(v[8 + 2 * i] * r, v[9 + 2 * i] * r); }
  u32x4* yr = (u32x4*)(y + (size_t)row * D_);
  yr[lane] = oa; yr[lane + 64] = ob;
}

template <int MODE, int BM, int BN, int WGM, int WGN, int STAGES>
struct Cfg {
  static constexpr int BK = 64;
  static constexpr int MF = BM / WGM / 32, NF = BN / WGN / 32;
  static constexpr int A_BYTES = BM * BK * 2;
  static constexpr int B_ROW = (MODE == 3) ? BN : BN * 2;
  static constexpr int B_SUB = (MODE == 3) ? BK * B_ROW + 128 : 0;
  static constexpr int B_BYTES = (MODE == 3) ? 2 * BK * B_ROW + 256 : BK * B_ROW;
  static constexpr int STAGE_BYTES = A_BYTES + B_BYTES;
  static constexpr int A_INSTR = A_BYTES / 1024 / 4;
  static constexpr int B_INSTR = BK * BN * 2 / 1024 / 4;
  static constexpr int L = A_INSTR + B_INSTR;
  static constexpr int LDS_BYTES = STAGES * STAGE_BYTES;
};

template <int ROWB> __device__ __forceinline__ int b_swz(int row) {
  if (ROWB % 256 == 0) return (row & 3) << 2;
  if (ROWB % 256 == 128) return ((row >> 1) & 1) << 2;
  return 0;
}

template <int MODE, int BM, int BN, int WGM, int WGN, int STAGES, int KTOT, bool TRANS_ALL>
__device__ __forceinline__ void gemm_main(f32x16 (&acc)[BM / WGM / 32][BN / WGN / 32], LAS char* lds, const u16* A, int lda,
                                          const u16* B0, const u16* B1, int ldb, int tile_n) {
  using C = Cfg<MODE, BM, BN, WGM, WGN, STAGES>;
  constexpr int MF = C::MF, NF = C::NF, NT = KTOT / 64;
  const int tid = threadIdx.x, lane = tid & 63;
  const int wave = __builtin_amdgcn_readfirstlane(tid >> 6);
  const int wm = wave / WGN, wn = wave % WGN;

  const rsrc_t ra = mk_rsrc(A, (unsigned)BM * lda * 2);
  const rsrc_t rb0 = mk_rsrc(B0, (unsigned)KTOT * ldb * 2);
  const rsrc_t rb1 = mk_rsrc(B1, (unsigned)KTOT * ldb * 2);

  unsigned a_src;
  {
    const int r = lane >> 3, cs = lane & 7, row = 8 * wave + r;
    a_src = (unsigned)(row * lda * 2 + ((cs ^ ((row >> 1) & 7)) << 4));
  }
  unsigned b_src[C::B_INSTR];
#pragma unroll
  for (int i = 0; i < C::B_INSTR; ++i) {
    if (MODE == 3) {
      const int piece = (wave & 1) * C::B_INSTR + i, ch = piece * 64 + lane;
      const int row = ch / 14, c = ch % 14;
      b_src[i] = (unsigned)(row * ldb * 2 + tile_n * 224 + c * 16);
    } else {
      constexpr int CPR = BN / 8;
      const int piece = wave * C::B_INSTR + i, ch = piece * 64 + lane;
      const int row = ch / CPR, cs = ch % CPR, c = cs ^ b_swz<C::B_ROW>(row);
      int col;
      if (MODE == 1) col = (c >> 3) * 1024 + tile_n * 64 + (c & 7) * 8;
      else col = tile_n * BN + c * 8;
      b_src[i] = (unsigned)(row * ldb * 2 + col * 2);
    }
  }
  auto stage = [&](int t) {
    const int slot = t % STAGES;
    LAS char* sa = lds + slot * C::STAGE_BYTES;
    LAS char* sb = sa + C::A_BYTES;
#pragma unroll
    for (int i = 0; i < C::A_INSTR; ++i)
      glds16(ra, sa + (wave + 4 * i) * 1024, a_src, (unsigned)(t * 128 + i * 32 * lda * 2));
    if (MODE == 3) {
      LAS char* sub = sb + (wave >> 1) * C::B_SUB + (wave & 1) * C::B_INSTR * 1024;
      if (wave < 2) {
#pragma unroll
        for (int i = 0; i < C::B_INSTR; ++i) glds16(rb0, sub + i * 1024, b_src[i], (unsigned)(t * 64 * ldb * 2));
      } else {
#pragma unroll
        for (int i = 0; i < C::B_INSTR; ++i) glds16(rb1, sub + i * 1024, b_src[i], (unsigned)(t * 64 * ldb * 2));
      }
    } else {
#pragma unroll
      for (int i = 0; i < C::B_INSTR; ++i)
        glds16(rb0, sb + (wave * C::B_INSTR + i) * 1024, b_src[i], (unsigned)(t * 64 * ldb * 2));
    }
  };

  const int h = lane >> 5, l31 = lane & 31;
  const int a_rd = (wm * MF * 32 + l31) * 128 + ((h ^ ((l31 >> 1) & 7)) << 4);
  const int grp = (lane >> 4) & 1, q = (lane & 15) >> 2, p = lane & 3, g = 4 * grp + p;
  const int pcol = 16 * (g >> 2) + 8 * (g & 1) + 4 * ((g >> 1) & 1);
  int b_rd;
  if (MODE == 3) {
    b_rd = (8 * h + q) * C::B_ROW + (pcol >> 4) * C::B_SUB + (pcol & 15) * 2;
  } else {
    b_rd = 0;
  }

  auto read_frags = [&](bf16x8 (&af)[MF], bf16x8 (&bfr)[NF], int slot, int s) {
    LAS char* sa = lds + slot * C::STAGE_BYTES;
    LAS char* sb = sa + C::A_BYTES;
#pragma unroll
    for (int n = 0; n < NF; ++n) {
      bf16x4 lo, hi;
      if (MODE == 3) {
        LAS char* pb = sb + b_rd + n * 32 + s * 16 * C::B_ROW;
        lo = __builtin_amdgcn_ds_read_tr16_b64_v4bf16((LAS bf16x4*)pb);
        hi = __builtin_amdgcn_ds_read_tr16_b64_v4bf16((LAS bf16x4*)(pb + 4 * C::B_ROW));
      } else {
        int col;
        if (MODE == 1) col = n * 64 + wn * 32 + pcol;
        else col = (wn * NF + n) * 32 + pcol;
        const int r0 = 16 * s + 8 * h + q, r1 = r0 + 4;
        const int o0 = r0 * C::B_ROW + ((((col >> 3) ^ b_swz<C::B_ROW>(r0))) << 4) + (col & 7) * 2;
        const int o1 = r1 * C::B_ROW + ((((col >> 3) ^ b_swz<C::B_ROW>(r1))) << 4) + (col & 7) * 2;
        lo = __builtin_amdgcn_ds_read_tr16_b64_v4bf16((LAS bf16x4*)(sb + o0));
        hi = __builtin_amdgcn_ds_read_tr16_b64_v4bf16((LAS bf16x4*)(sb + o1));
      }
      bfr[n] = __builtin_shufflevector(lo, hi, 0, 1, 2, 3, 4, 5, 6, 7);
    }
#pragma unroll
    for (int m = 0; m < MF; ++m) af[m] = *(LAS bf16x8*)(sa + (a_rd ^ (s << 5)) + m * 4096);
  };
  auto mma = [&](bf16x8 (&af)[MF], bf16x8 (&bfr)[NF]) {
#pragma unroll
    for (int n = 0; n < NF; ++n)
#pragma unroll
      for (int m = 0; m < MF; ++m) {
        if (TRANS_ALL || n == 0) acc[m][n] = __builtin_amdgcn_mfma_f32_32x32x16_bf16(bfr[n], af[m], acc[m][n], 0, 0, 0);
        else acc[m][n] = __builtin_amdgcn_mfma_f32_32x32x16_bf16(af[m], bfr[n], acc[m][n], 0, 0, 0);
      }
  };

#pragma unroll
  for (int m = 0; m < MF; ++m)
#pragma unroll
    for (int n = 0; n < NF; ++n)
#pragma unroll
      for (int i = 0; i < 16; ++i) acc[m][n][i] = 0.f;

  bf16x8 a0[MF], b0[NF], a1[MF], b1[NF];
#pragma unroll
  for (int t = 0; t < STAGES; ++t) stage(t);
  WAIT_V((STAGES - 1) * C::L);
  BAR();
  read_frags(a0, b0, 0, 0);

  int slot = 0;
#pragma unroll 1
  for (int t = 0; t < NT; ++t) {
    read_frags(a1, b1, slot, 1);
    mma(a0, b0);
    read_frags(a0, b0, slot, 2);
    mma(a1, b1);
    read_frags(a1, b1, slot, 3);
    mma(a0, b0);
    const int nslot = (slot + 1 == STAGES) ? 0 : slot + 1;
    if (t + 1 < NT) {
      WAIT_L0();
      if (t + STAGES <= NT) { WAIT_V((STAGES - 2) * C::L); } else { WAIT_V(0); }
      BAR();
      read_frags(a0, b0, nslot, 0);
      if (t + STAGES < NT) stage(t + STAGES);
    }
    mma(a1, b1);
    slot = nslot;
  }
}

__device__ __forceinline__ float phi(float t) {
  float r = t > 0.f ? rbf(t + 1.f) : rbf(__expf(t));
  return rbf(r + 1e-6f);
}

using Cfg1 = Cfg<1, 128, 192, 2, 2, 3>;
__global__ __launch_bounds__(256, 1) void gemm1_kernel(const u16* __restrict__ xn, const u16* __restrict__ wqkv, u16* __restrict__ pq,
                                                       float* __restrict__ kvp) {
  extern __shared__ __attribute__((aligned(16))) char smem[];
  LAS char* lds = (LAS char*)smem;
  const int b = blockIdx.x, xcd = b & 7, idx = b >> 3;
  const int tm = (xcd >> 2) * 8 + (idx & 7), tn = (xcd & 3) * 4 + (idx >> 3);
  f32x16 acc[2][3];
  gemm_main<1, 128, 192, 2, 2, 3, 1024, false>(acc, lds, xn + (size_t)tm * 128 * D_, D_, wqkv, wqkv, 3 * D_, tn);
  const int lane = threadIdx.x & 63, wave = __builtin_amdgcn_readfirstlane(threadIdx.x >> 6);
  const int wm = wave >> 1, wn = wave & 1, h = lane >> 5, l31 = lane & 31;
  const int head = tn * 2 + wn;
#pragma unroll
  for (int m = 0; m < 2; ++m) {
    u32x4 lo, hi;
#pragma unroll
    for (int i = 0; i < 4; ++i) {
      lo[i] = pack_bf16(phi(rbf(acc[m][0][2 * i])), phi(rbf(acc[m][0][2 * i + 1])));
      hi[i] = pack_bf16(phi(rbf(acc[m][0][8 + 2 * i])), phi(rbf(acc[m][0][9 + 2 * i])));
    }
    u16* dst = pq + (size_t)(tm * 128 + wm * 64 + m * 32 + l31) * D_ + head * 32 + 8 * h;
    *(u32x4*)dst = lo;
    *(u32x4*)(dst + 16) = hi;
  }
  f32x16 kv;
#pragma unroll
  for (int i = 0; i < 16; ++i) kv[i] = 0.f;
  float z = 0.f;
#pragma unroll
  for (int m = 0; m < 2; ++m)
#pragma unroll
    for (int s = 0; s < 2; ++s) {
      u32x4 ka, va;
#pragma unroll
      for (int i = 0; i < 4; ++i) {
        const float k0 = phi(rbf(acc[m][1][8 * s + 2 * i])), k1 = phi(rbf(acc[m][1][8 * s + 2 * i + 1]));
        z += k0 + k1;
        ka[i] = pack_bf16(k0, k1);
        va[i] = pack_bf16(acc[m][2][8 * s + 2 * i], acc[m][2][8 * s + 2 * i + 1]);
      }
      kv = __builtin_amdgcn_mfma_f32_32x32x16_bf16(__builtin_bit_cast(bf16x8, ka), __builtin_bit_cast(bf16x8, va), kv, 0, 0, 0);
    }
  z += __shfl_xor(z, 32);
  float* dst = kvp + ((size_t)head * 32 + tm * 2 + wm) * (17 * 64);
#pragma unroll
  for (int i = 0; i < 16; ++i) dst[i * 64 + lane] = kv[i];
  dst[16 * 64 + lane] = z;
}

__global__ __launch_bounds__(256) void o_kernel(const u16* __restrict__ pq, const float* __restrict__ kvp, u16* __restrict__ o) {
  __shared__ float red[4][17 * 64];
  const int head = blockIdx.x & 31, rg = blockIdx.x >> 5;
  const int lane = threadIdx.x & 63, wave = threadIdx.x >> 6, h = lane >> 5, l31 = lane & 31;
  u32x4 qf[2][2];
#pragma unroll
  for (int m = 0; m < 2; ++m) {
    const u16* src = pq + (size_t)(rg * 256 + wave * 64 + m * 32 + l31) * D_ + head * 32 + 8 * h;
    qf[m][0] = *(const u32x4*)src;
    qf[m][1] = *(const u32x4*)(src + 16);
  }
  {
    float s[17];
#pragma unroll
    for (int i = 0; i < 17; ++i) s[i] = 0.f;
    const float* src = kvp + ((size_t)head * 32 + wave * 8) * (17 * 64) + lane;
#pragma unroll
    for (int pI = 0; pI < 8; ++pI)
#pragma unroll
      for (int i = 0; i < 17; ++i) s[i] += src[(pI * 17 + i) * 64];
#pragma unroll
    for (int i = 0; i < 17; ++i) red[wave][i * 64 + lane] = s[i];
  }
  __syncthreads();
  u32x4 kvf[2];
#pragma unroll
  for (int s = 0; s < 2; ++s)
#pragma unroll
    for (int i = 0; i < 4; ++i) {
      const int r0 = (8 * s + 2 * i) * 64 + lane, r1 = r0 + 64;
      kvf[s][i] = pack_bf16(red[0][r0] + red[1][r0] + red[2][r0] + red[3][r0], red[0][r1] + red[1][r1] + red[2][r1] + red[3][r1]);
    }
  float zz[16];
#pragma unroll
  for (int j = 0; j < 16; ++j) {
    const int dhi = j >> 3, jj = j & 7;
    const int g = 4 * dhi + 2 * (jj >> 2) + h, slot = 4 * g + (jj & 3);
    const int r = 16 * 64 + slot;
    zz[j] = rbf(red[0][r] + red[1][r] + red[2][r] + red[3][r]);
  }
#pragma unroll
  for (int m = 0; m < 2; ++m) {
    f32x16 num;
#pragma unroll
    for (int i = 0; i < 16; ++i) num[i] = 0.f;
    num = __builtin_amdgcn_mfma_f32_32x32x16_bf16(__builtin_bit_cast(bf16x8, kvf[0]), __builtin_bit_cast(bf16x8, qf[m][0]), num, 0, 0, 0);
    num = __builtin_amdgcn_mfma_f32_32x32x16_bf16(__builtin_bit_cast(bf16x8, kvf[1]), __builtin_bit_cast(bf16x8, qf[m][1]), num, 0, 0, 0);
    float den = 0.f;
#pragma unroll
    for (int s = 0; s < 2; ++s)
#pragma unroll
      for (int i = 0; i < 4; ++i) den += bf_lo(qf[m][s][i]) * zz[8 * s + 2 * i] + bf_hi(qf[m][s][i]) * zz[8 * s + 2 * i + 1];
    den += __shfl_xor(den, 32);
    den = rbf(rbf(den) + 1e-6f);
    const float inv = 1.f / den;
    u32x4 lo, hi;
#pragma unroll
    for (int i = 0; i < 4; ++i) {
      lo[i] = pack_bf16(rbf(num[2 * i]) * inv, rbf(num[2 * i + 1]) * inv);
      hi[i] = pack_bf16(rbf(num[8 + 2 * i]) * inv, rbf(num[9 + 2 * i]) * inv);
    }
    u16* dst = o + (size_t)(rg * 256 + wave * 64 + m * 32 + l31) * D_ + head * 32 + 8 * h;
    *(u32x4*)dst = lo;
    *(u32x4*)(dst + 16) = hi;
  }
}

template <int KTOT, int STAGES>
__global__ __launch_bounds__(256, 1) void gemm_res_kernel(const u16* __restrict__ a, const u16* __restrict__ w, const u16* __restrict__ res,
                                                          u16* __restrict__ out) {
  extern __shared__ __attribute__((aligned(16))) char smem[];
  LAS char* lds = (LAS char*)smem;
  const int b = blockIdx.x, xcd = b & 7, idx = b >> 3;
  const int tm = (xcd >> 1) * 4 + (idx & 3), tn = (xcd & 1) * 8 + (idx >> 2);
  f32x16 acc[2][1];
  gemm_main<2, 128, 64, 2, 2, STAGES, KTOT, true>(acc, lds, a + (size_t)tm * 128 * KTOT, KTOT, w, w, D_, tn);
  const int lane = threadIdx.x & 63, wave = __builtin_amdgcn_readfirstlane(threadIdx.x >> 6);
  const int wm = wave >> 1, wn = wave & 1, h = lane >> 5, l31 = lane & 31;
#pragma unroll
  for (int m = 0; m < 2; ++m) {
    const size_t off = (size_t)(tm * 128 + wm * 64 + m * 32 + l31) * D_ + tn * 64 + wn * 32 + 8 * h;
    const u32x4 r0 = *(const u32x4*)(res + off), r1 = *(const u32x4*)(res + off + 16);
    u32x4 lo, hi;
#pragma unroll
    for (int i = 0; i < 4; ++i) {
      lo[i] = pack_bf16(bf_lo(r0[i]) + rbf(acc[m][0][2 * i]), bf_hi(r0[i]) + rbf(acc[m][0][2 * i + 1]));
      hi[i] = pack_bf16(bf_lo(r1[i]) + rbf(acc[m][0][8 + 2 * i]), bf_hi(r1[i]) + rbf(acc[m][0][9 + 2 * i]));
    }
    *(u32x4*)(out + off) = lo;
    *(u32x4*)(out + off + 16) = hi;
  }
}

__device__ __forceinline__ float gelu_tanh(float x) {
  const float u = x * (1.f + 0.044715f * x * x) * (-2.f * 0.7978845608028654f * 1.4426950408889634f);
  return x * __builtin_amdgcn_rcpf(1.f + __builtin_amdgcn_exp2f(u));
}
using Cfg3 = Cfg<3, 256, 224, 4, 1, 2>;
__global__ __launch_bounds__(256, 1) void gemm3_kernel(const u16* __restrict__ hn, const u16* __restrict__ wg, const u16* __restrict__ wu,
                                                       u16* __restrict__ g) {
  extern __shared__ __attribute__((aligned(16))) char smem[];
  LAS char* lds = (LAS char*)smem;
  const int b = blockIdx.x, xcd = b & 7, idx = b >> 3;
  const int tm = idx & 7, tn = xcd * 4 + (idx >> 3);
  f32x16 acc[2][7];
  gemm_main<3, 256, 224, 4, 1, 2, 1024, true>(acc, lds, hn + (size_t)tm * 256 * D_, D_, wg, wu, F_, tn);
  const int lane = threadIdx.x & 63, wave = __builtin_amdgcn_readfirstlane(threadIdx.x >> 6);
  const int h = lane >> 5, l31 = lane & 31;
#pragma unroll
  for (int m = 0; m < 2; ++m) {
    u16* dst = g + (size_t)(tm * 256 + wave * 64 + m * 32 + l31) * F_ + tn * 112 + 8 * h;
#pragma unroll
    for (int n = 0; n < 7; ++n) {
      u32x4 o;
#pragma unroll
      for (int i = 0; i < 4; ++i) {
        const float g0 = rbf(gelu_tanh(rbf(acc[m][n][2 * i]))) * rbf(acc[m][n][8 + 2 * i]);
        const float g1 = rbf(gelu_tanh(rbf(acc[m][n][2 * i + 1]))) * rbf(acc[m][n][9 + 2 * i]);
        o[i] = pack_bf16(g0, g1);
      }
      *(u32x4*)(dst + n * 16) = o;
    }
  }
}

extern "C" void kernel_launch(void* const* d_in, const int* in_sizes, int n_in, void* d_out, int out_size, void* d_ws, size_t ws_size,
                              hipStream_t stream) {
  const u16* x = (const u16*)d_in[0];
  const u16* wqkv = (const u16*)d_in[1];
  const u16* wo = (const u16*)d_in[2];
  const u16* wg = (const u16*)d_in[4];
  const u16* wu = (const u16*)d_in[5];
  const u16* wd = (const u16*)d_in[6];
  u16* out = (u16*)d_out;
  char* ws = (char*)d_ws;
  const size_t MB = 1 << 20;
  u16* xn = (u16*)(ws);
  u16* pq = (u16*)(ws + 4 * MB);
  u16* o = (u16*)(ws + 8 * MB);
  u16* hbuf = (u16*)(ws + 12 * MB);
  u16* hn = (u16*)(ws + 16 * MB);
  float* kvp = (float*)(ws + 20 * MB);
  u16* g = (u16*)(ws + 28 * MB);

  using Cfg2 = Cfg<2, 128, 64, 2, 2, 4>;
  hipFuncSetAttribute((const void*)gemm1_kernel, hipFuncAttributeMaxDynamicSharedMemorySize, Cfg1::LDS_BYTES);
  hipFuncSetAttribute((const void*)gemm3_kernel, hipFuncAttributeMaxDynamicSharedMemorySize, Cfg3::LDS_BYTES);
  hipFuncSetAttribute((const void*)gemm_res_kernel<1024, 4>, hipFuncAttributeMaxDynamicSharedMemorySize, Cfg2::LDS_BYTES);
  hipFuncSetAttribute((const void*)gemm_res_kernel<3584, 4>, hipFuncAttributeMaxDynamicSharedMemorySize, Cfg2::LDS_BYTES);

  ln_kernel<<<S_ / 4, 256, 0, stream>>>(x, xn);
  gemm1_kernel<<<256, 256, Cfg1::LDS_BYTES, stream>>>(xn, wqkv, pq, kvp);
  o_kernel<<<256, 256, 0, stream>>>(pq, kvp, o);
  gemm_res_kernel<1024, 4><<<256, 256, Cfg2::LDS_BYTES, stream>>>(o, wo, x, hbuf);
  ln_kernel<<<S_ / 4, 256, 0, stream>>>(hbuf, hn);
  gemm3_kernel<<<256, 256, Cfg3::LDS_BYTES, stream>>>(hn, wg, wu, g);
  gemm_res_kernel<3584, 4><<<256, 256, Cfg2::LDS_BYTES, stream>>>(g, wd, hbuf, out);
}
```

```cpp
#include <hip/hip_runtime.h>
#include <hip/hip_bf16.h>
#include <stdint.h>
#include <type_traits>

#define LAS __attribute__((address_space(3)))
typedef unsigned short u16;
typedef __bf16 bf16x4 __attribute__((ext_vector_type(4)));
typedef __bf16 bf16x8 __attribute__((ext_vector_type(8)));
typedef float f32x16 __attribute__((ext_vector_type(16)));
typedef float f32x4 __attribute__((ext_vector_type(4)));
typedef unsigned u32x4 __attribute__((ext_vector_type(4)));
typedef __bf16 bf16x2 __attribute__((ext_vector_type(2)));

constexpr int S_ = 2048, D_ = 1024, F_ = 3584;

__device__ __forceinline__ unsigned pack_bf16(float lo, float hi) {
  bf16x2 v; v[0] = (__bf16)lo; v[1] = (__bf16)hi;
  return __builtin_bit_cast(unsigned, v);
}
__device__ __forceinline__ float rbf(float x) { return (float)(__bf16)x; }
__device__ __forceinline__ float bf_lo(unsigned u) { return __builtin_bit_cast(float, u << 16); }
__device__ __forceinline__ float bf_hi(unsigned u) { return __builtin_bit_cast(float, u & 0xffff0000u); }

#define WAIT_V(n) asm volatile("s_waitcnt vmcnt(%0)" ::"i"(n) : "memory")
#define WAIT_L0() asm volatile("s_waitcnt lgkmcnt(0)" ::: "memory")
#define BAR() asm volatile("s_barrier" ::: "memory")
#define SB() __builtin_amdgcn_sched_barrier(0)

__device__ __forceinline__ u32x4 mk_srd(const void* p, unsigned bytes) {
  const unsigned long long a = (unsigned long long)p;
  u32x4 r;
  r[0] = __builtin_amdgcn_readfirstlane((unsigned)a);
  r[1] = __builtin_amdgcn_readfirstlane((unsigned)(a >> 32));
  r[2] = bytes;
  r[3] = 0x00020000u;
  return r;
}
__device__ __forceinline__ void glds16(const u32x4& srd, unsigned lds_dst, unsigned voff, unsigned soff) {
  asm volatile("s_mov_b32 m0, %0\n\ts_nop 0\n\tbuffer_load_dwordx4 %1, %2, %3 offen lds" ::"s"(lds_dst), "v"(voff), "s"(srd), "s"(soff) : "memory");
}
template <int IMM>
__device__ __forceinline__ void gld16(bf16x8& dst, const u32x4& srd, unsigned voff, unsigned soff) {
  asm volatile("buffer_load_dwordx4 %0, %1, %2, %3 offen offset:%4" : "=v"(dst) : "v"(voff), "s"(srd), "s"(soff), "i"(IMM) : "memory");
}
__device__ __forceinline__ bf16x8 tr_frag(LAS char* p0, LAS char* p1) {
  const bf16x4 lo = __builtin_amdgcn_ds_read_tr16_b64_v4bf16((LAS bf16x4*)p0);
  const bf16x4 hi = __builtin_amdgcn_ds_read_tr16_b64_v4bf16((LAS bf16x4*)p1);
  return __builtin_shufflevector(lo, hi, 0, 1, 2, 3, 4, 5, 6, 7);
}
#define MFMA(a, b, c) __builtin_amdgcn_mfma_f32_32x32x16_bf16(a, b, c, 0, 0, 0)

template <int ROWB> __device__ __forceinline__ int b_swz(int row) {
  if (ROWB % 256 == 0) return (row & 3) << 2;
  if (ROWB % 256 == 128) return ((row >> 1) & 1) << 2;
  return 0;
}

template <bool FRAG_IN>
__global__ __launch_bounds__(512) void ln_kernel(const u16* __restrict__ x, u16* __restrict__ y) {
  __shared__ float red[2][8][32];
  const int lane = threadIdx.x & 63, wave = threadIdx.x >> 6, mb = blockIdx.x, h = lane >> 5, l31 = lane & 31;
  u32x4 v[8];
#pragma unroll
  for (int i = 0; i < 8; ++i) {
    const int kb = wave * 8 + i;
    if (FRAG_IN) v[i] = *(const u32x4*)(x + ((size_t)(mb * 64 + kb) * 512 + lane * 8));
    else v[i] = *(const u32x4*)(x + (size_t)(mb * 32 + l31) * D_ + kb * 16 + h * 8);
  }
  float s = 0.f;
#pragma unroll
  for (int i = 0; i < 8; ++i)
#pragma unroll
    for (int j = 0; j < 4; ++j) s += bf_lo(v[i][j]) + bf_hi(v[i][j]);
  s += __shfl_xor(s, 32);
  if (h == 0) red[0][wave][l31] = s;
  __syncthreads();
  float m = 0.f;
#pragma unroll
  for (int w = 0; w < 8; ++w) m += red[0][w][l31];
  m *= (1.f / D_);
  float q = 0.f;
#pragma unroll
  for (int i = 0; i < 8; ++i)
#pragma unroll
    for (int j = 0; j < 4; ++j) { const float a = bf_lo(v[i][j]) - m, b = bf_hi(v[i][j]) - m; q += a * a + b * b; }
  q += __shfl_xor(q, 32);
  if (h == 0) red[1][wave][l31] = q;
  __syncthreads();
  float var = 0.f;
#pragma unroll
  for (int w = 0; w < 8; ++w) var += red[1][w][l31];
  const float r = 1.f / sqrtf(var * (1.f / D_) + 1e-5f);
#pragma unroll
  for (int i = 0; i < 8; ++i) {
    u32x4 o;
#pragma unroll
    for (int j = 0; j < 4; ++j) o[j] = pack_bf16((bf_lo(v[i][j]) - m) * r, (bf_hi(v[i][j]) - m) * r);
    *(u32x4*)(y + ((size_t)(mb * 64 + wave * 8 + i) * 512 + lane * 8)) = o;
  }
}

template <int MODE, int BN, int STAGES>
struct SCfg {
  static constexpr int B_ROW = (MODE == 3) ? BN : BN * 2;
  static constexpr int B_SUB = (MODE == 3) ? 64 * B_ROW + 128 : 0;
  static constexpr int STAGE_BYTES = (MODE == 3) ? 2 * 64 * B_ROW + 256 : 64 * B_ROW;
  static constexpr int B_INSTR = 64 * BN * 2 / 1024 / 4;
  static constexpr int LDS_BYTES = STAGES * STAGE_BYTES;
};

template <int MODE, int MF, int NF, int STAGES, int KTOT>
__device__ __forceinline__ void gemm_shared(f32x16 (&acc)[MF][NF], LAS char* lds, const u16* A  ,
                                            const u16* B0, const u16* B1, int ldb, int tile_n) {
  constexpr int BN = NF * 32;
  using C = SCfg<MODE, BN, STAGES>;
  constexpr int NT = KTOT / 64, BI = C::B_INSTR, NMM = MF * NF;
  const int tid = threadIdx.x, lane = tid & 63;
  const int wave = __builtin_amdgcn_readfirstlane(tid >> 6);
  const unsigned lds0 = (unsigned)(size_t)lds;

  const u32x4 sa = mk_srd(A + (size_t)wave * MF * 32 * KTOT, (unsigned)MF * 32 * KTOT * 2);
  const u32x4 sb = mk_srd((MODE == 3 && wave >= 2) ? B1 : B0, (unsigned)KTOT * ldb * 2);

  unsigned a_off[MF];
#pragma unroll
  for (int m = 0; m < MF; ++m) a_off[m] = lane * 16 + m * (KTOT * 64);
  unsigned b_src[BI];
#pragma unroll
  for (int i = 0; i < BI; ++i) {
    if (MODE == 3) {
      const int ch = ((wave & 1) * BI + i) * 64 + lane, row = ch / 14, c = ch % 14;
      b_src[i] = (unsigned)(row * ldb * 2 + tile_n * 224 + c * 16);
    } else {
      constexpr int CPR = BN / 8;
      const int ch = (wave * BI + i) * 64 + lane, row = ch / CPR, cs = ch % CPR, c = cs ^ b_swz<C::B_ROW>(row);
      const int col = (c >> 3) * 1024 + tile_n * 64 + (c & 7) * 8;
      b_src[i] = (unsigned)(row * ldb * 2 + col * 2);
    }
  }
  const unsigned b_dst = lds0 + ((MODE == 3) ? (wave >> 1) * C::B_SUB + (wave & 1) * BI * 1024 : wave * BI * 1024);

  const int h = lane >> 5;
  const int grp = (lane >> 4) & 1, q = (lane & 15) >> 2, p = lane & 3, g = 4 * grp + p;
  const int pcol = 16 * (g >> 2) + 8 * (g & 1) + 4 * ((g >> 1) & 1);
  int b_rd0, b_rd1;
  if (MODE == 3) {
    b_rd0 = (8 * h + q) * C::B_ROW + (pcol >> 4) * C::B_SUB + (pcol & 15) * 2;
    b_rd1 = b_rd0 + 4 * C::B_ROW;
  } else {
    const int r0 = 8 * h + q, r1 = r0 + 4;
    b_rd0 = r0 * C::B_ROW + (((pcol >> 3) ^ b_swz<C::B_ROW>(r0)) << 4) + (pcol & 7) * 2;
    b_rd1 = r1 * C::B_ROW + (((pcol >> 3) ^ b_swz<C::B_ROW>(r1)) << 4) + (pcol & 7) * 2;
  }
  auto frag_off = [&](int rd, int n, int s) -> int {
    if (MODE == 3) return rd + n * 32 + s * 16 * C::B_ROW;
    return (rd ^ ((n & 1) << 6)) + (n >> 1) * 128 + s * 16 * C::B_ROW;
  };

#pragma unroll
  for (int m = 0; m < MF; ++m)
#pragma unroll
    for (int n = 0; n < NF; ++n)
#pragma unroll
      for (int i = 0; i < 16; ++i) acc[m][n][i] = 0.f;

  bf16x8 ar[8][MF], bf[2][NF];
#pragma unroll
  for (int i = 0; i < BI; ++i) glds16(sb, b_dst + i * 1024, b_src[i], 0);
#pragma unroll
  for (int m = 0; m < MF; ++m) { gld16<0>(ar[0][m], sa, a_off[m], 0); }
#pragma unroll
  for (int m = 0; m < MF; ++m) { gld16<1024>(ar[1][m], sa, a_off[m], 0); }
#pragma unroll
  for (int m = 0; m < MF; ++m) { gld16<2048>(ar[2][m], sa, a_off[m], 0); }
#pragma unroll
  for (int m = 0; m < MF; ++m) { gld16<3072>(ar[3][m], sa, a_off[m], 0); }
#pragma unroll
  for (int i = 0; i < BI; ++i) glds16(sb, b_dst + C::STAGE_BYTES + i * 1024, b_src[i], 64 * ldb * 2);
#pragma unroll
  for (int m = 0; m < MF; ++m) { gld16<0>(ar[4][m], sa, a_off[m], 4096); }
#pragma unroll
  for (int m = 0; m < MF; ++m) { gld16<1024>(ar[5][m], sa, a_off[m], 4096); }
#pragma unroll
  for (int m = 0; m < MF; ++m) { gld16<2048>(ar[6][m], sa, a_off[m], 4096); }
#pragma unroll
  for (int m = 0; m < MF; ++m) { gld16<3072>(ar[7][m], sa, a_off[m], 4096); }
#pragma unroll
  for (int t = 2; t < STAGES; ++t)
#pragma unroll
    for (int i = 0; i < BI; ++i) glds16(sb, b_dst + t * C::STAGE_BYTES + i * 1024, b_src[i], t * 64 * ldb * 2);
  WAIT_V(8 * MF + (STAGES - 1) * BI);
  BAR();
#pragma unroll
  for (int n = 0; n < NF; ++n) bf[0][n] = tr_frag(lds + frag_off(b_rd0, n, 0), lds + frag_off(b_rd1, n, 0));
  SB();

  int slot = 0;
  auto step = [&](auto PC, auto SC, int t, bool first) {
    constexpr int P = decltype(PC)::value, s = decltype(SC)::value, R = 4 * P + s, cur = s & 1, nxt = cur ^ 1;
    constexpr int NB = (s == 3) ? 1 : 2;
    if (first) {
      constexpr int later = (R < 4) ? ((7 - R) * MF + (STAGES - 1) * BI) : ((7 - R) * MF + (STAGES - 2) * BI);
      constexpr int issued = R * MF + (R > 3 ? BI : 0);
      WAIT_V(later + issued > 63 ? 63 : later + issued);
    } else {
      WAIT_V(7 * MF + NB * BI);
    }
#pragma unroll
    for (int m = 0; m < MF; ++m) asm volatile("" : "+v"(ar[R][m]));
    int nslot = slot;
    if (s == 3) {
      WAIT_L0();
      if (first) {
        if (P == 0) { WAIT_V(4 * MF + (STAGES - 2) * BI + 3 * MF); }
        else { WAIT_V((STAGES > 3 ? (STAGES - 3) * BI : 0) + BI + 7 * MF); }
      } else {
        constexpr int y = (STAGES - 2) * BI + (4 * (STAGES - 1)) * MF;
        WAIT_V(y > 7 * MF + NB * BI ? 7 * MF + NB * BI : y);
      }
      BAR();
      nslot = (slot + 1 == STAGES) ? 0 : slot + 1;
    }
    LAS char* rbase = lds + nslot * C::STAGE_BYTES;
    constexpr int ns = (s + 1) & 3;
    const unsigned gdst = b_dst + slot * C::STAGE_BYTES;
    const unsigned gsoff = (unsigned)(t + STAGES) * 64 * ldb * 2;
    SB();
#pragma unroll
    for (int i = 0; i < NMM; ++i) {
      const int n = i / MF, m = i % MF;
      if (MODE == 3 || n < 2) acc[m][n] = MFMA(bf[cur][n], ar[R][m], acc[m][n]);
      else acc[m][n] = MFMA(ar[R][m], bf[cur][n], acc[m][n]);
      SB();
      if (i < NF) bf[nxt][i] = tr_frag(rbase + frag_off(b_rd0, i, ns), rbase + frag_off(b_rd1, i, ns));
      if (s == 3) {
        constexpr int G0 = (NMM >= NF + BI) ? NF : 0;
        if (i >= G0 && i - G0 < BI) glds16(sb, gdst + (i - G0) * 1024, b_src[i - G0], gsoff);
      }
      SB();
    }
#pragma unroll
    for (int m = 0; m < MF; ++m) gld16<s * 1024>(ar[R][m], sa, a_off[m], (unsigned)(t + 2) * 4096);
    SB();
    slot = nslot;
  };
  using I0 = std::integral_constant<int, 0>; using I1 = std::integral_constant<int, 1>;
  using I2 = std::integral_constant<int, 2>; using I3 = std::integral_constant<int, 3>;
  step(I0{}, I0{}, 0, true); step(I0{}, I1{}, 0, true); step(I0{}, I2{}, 0, true); step(I0{}, I3{}, 0, true);
  step(I1{}, I0{}, 1, true); step(I1{}, I1{}, 1, true); step(I1{}, I2{}, 1, true); step(I1{}, I3{}, 1, true);
#pragma unroll 1
  for (int t = 2; t < NT; t += 2) {
    step(I0{}, I0{}, t, false); step(I0{}, I1{}, t, false); step(I0{}, I2{}, t, false); step(I0{}, I3{}, t, false);
    step(I1{}, I0{}, t + 1, false); step(I1{}, I1{}, t + 1, false); step(I1{}, I2{}, t + 1, false); step(I1{}, I3{}, t + 1, false);
  }
  WAIT_V(0);
#pragma unroll
  for (int r = 0; r < 8; ++r)
#pragma unroll
    for (int m = 0; m < MF; ++m) asm volatile("" : "+v"(ar[r][m]));
  WAIT_L0();
}

__device__ __forceinline__ float phi(float t) {
  float r = t > 0.f ? rbf(t + 1.f) : rbf(__expf(t));
  return rbf(r + 1e-6f);
}

constexpr int ST1 = 5;
using SCfg1 = SCfg<1, 192, ST1>;
__global__ __launch_bounds__(256, 1) void gemm1_kernel(const u16* __restrict__ xn, const u16* __restrict__ wqkv, u16* __restrict__ pq,
                                                       float* __restrict__ kvp) {
  extern __shared__ __attribute__((aligned(16))) char smem[];
  LAS char* lds = (LAS char*)smem;
  const int b = blockIdx.x, xcd = b & 7, idx = b >> 3;
  const int tm = (xcd >> 2) * 8 + (idx & 7), tn = (xcd & 3) * 4 + (idx >> 3);
  f32x16 acc[1][6];
  gemm_shared<1, 1, 6, ST1, 1024>(acc, lds, xn + (size_t)tm * 128 * D_, wqkv, wqkv, 3 * D_, tn);
  const int lane = threadIdx.x & 63, wave = __builtin_amdgcn_readfirstlane(threadIdx.x >> 6);
  const int mb = tm * 4 + wave;
#pragma unroll
  for (int hh = 0; hh < 2; ++hh) {
    const int head = tn * 2 + hh;
    u32x4 lo, hi;
#pragma unroll
    for (int i = 0; i < 4; ++i) {
      lo[i] = pack_bf16(phi(rbf(acc[0][hh][2 * i])), phi(rbf(acc[0][hh][2 * i + 1])));
      hi[i] = pack_bf16(phi(rbf(acc[0][hh][8 + 2 * i])), phi(rbf(acc[0][hh][9 + 2 * i])));
    }
    u16* dst = pq + ((size_t)(mb * 64 + head * 2) * 512 + lane * 8);
    *(u32x4*)dst = lo;
    *(u32x4*)(dst + 512) = hi;
  }
  BAR();
  LAS float* red = (LAS float*)lds;
#pragma unroll
  for (int hh = 0; hh < 2; ++hh) {
    f32x16 kv;
#pragma unroll
    for (int i = 0; i < 16; ++i) kv[i] = 0.f;
    float z = 0.f;
#pragma unroll
    for (int s = 0; s < 2; ++s) {
      u32x4 ka, va;
#pragma unroll
      for (int i = 0; i < 4; ++i) {
        const float k0 = phi(rbf(acc[0][2 + hh][8 * s + 2 * i])), k1 = phi(rbf(acc[0][2 + hh][8 * s + 2 * i + 1]));
        z += k0 + k1;
        ka[i] = pack_bf16(k0, k1);
        va[i] = pack_bf16(acc[0][4 + hh][8 * s + 2 * i], acc[0][4 + hh][8 * s + 2 * i + 1]);
      }
      kv = MFMA(__builtin_bit_cast(bf16x8, ka), __builtin_bit_cast(bf16x8, va), kv);
    }
    z += __shfl_xor(z, 32);
#pragma unroll
    for (int i = 0; i < 16; ++i) red[((wave * 2 + hh) * 17 + i) * 64 + lane] = kv[i];
    red[((wave * 2 + hh) * 17 + 16) * 64 + lane] = z;
  }
  __syncthreads();
  for (int e = threadIdx.x; e < 2 * 17 * 64; e += 256) {
    const float s = red[e] + red[2176 + e] + red[2 * 2176 + e] + red[3 * 2176 + e];
    const int hh = e / (17 * 64), r = e % (17 * 64);
    kvp[((size_t)(tn * 2 + hh) * 16 + tm) * (17 * 64) + r] = s;
  }
}

__global__ __launch_bounds__(256) void o_kernel(const u16* __restrict__ pq, const float* __restrict__ kvp, u16* __restrict__ o) {
  __shared__ float red[4][17 * 64];
  const int head = blockIdx.x & 31, rg = blockIdx.x >> 5;
  const int lane = threadIdx.x & 63, wave = threadIdx.x >> 6, h = lane >> 5;
  u32x4 qf[2][2];
#pragma unroll
  for (int m = 0; m < 2; ++m) {
    const u16* src = pq + ((size_t)((rg * 8 + wave * 2 + m) * 64 + head * 2) * 512 + lane * 8);
    qf[m][0] = *(const u32x4*)src;
    qf[m][1] = *(const u32x4*)(src + 512);
  }
  {
    float s[17];
#pragma unroll
    for (int i = 0; i < 17; ++i) s[i] = 0.f;
    const float* src = kvp + ((size_t)head * 16 + wave * 4) * (17 * 64) + lane;
#pragma unroll
    for (int pI = 0; pI < 4; ++pI)
#pragma unroll
      for (int i = 0; i < 17; ++i) s[i] += src[(pI * 17 + i) * 64];
#pragma unroll
    for (int i = 0; i < 17; ++i) red[wave][i * 64 + lane] = s[i];
  }
  __syncthreads();
  u32x4 kvf[2];
#pragma unroll
  for (int s = 0; s < 2; ++s)
#pragma unroll
    for (int i = 0; i < 4; ++i) {
      const int r0 = (8 * s + 2 * i) * 64 + lane, r1 = r0 + 64;
      kvf[s][i] = pack_bf16(red[0][r0] + red[1][r0] + red[2][r0] + red[3][r0], red[0][r1] + red[1][r1] + red[2][r1] + red[3][r1]);
    }
  float zz[16];
#pragma unroll
  for (int j = 0; j < 16; ++j) {
    const int dhi = j >> 3, jj = j & 7;
    const int g = 4 * dhi + 2 * (jj >> 2) + h, slot = 4 * g + (jj & 3);
    const int r = 16 * 64 + slot;
    zz[j] = rbf(red[0][r] + red[1][r] + red[2][r] + red[3][r]);
  }
#pragma unroll
  for (int m = 0; m < 2; ++m) {
    f32x16 num;
#pragma unroll
    for (int i = 0; i < 16; ++i) num[i] = 0.f;
    num = MFMA(__builtin_bit_cast(bf16x8, kvf[0]), __builtin_bit_cast(bf16x8, qf[m][0]), num);
    num = MFMA(__builtin_bit_cast(bf16x8, kvf[1]), __builtin_bit_cast(bf16x8, qf[m][1]), num);
    float den = 0.f;
#pragma unroll
    for (int s = 0; s < 2; ++s)
#pragma unroll
      for (int i = 0; i < 4; ++i) den += bf_lo(qf[m][s][i]) * zz[8 * s + 2 * i] + bf_hi(qf[m][s][i]) * zz[8 * s + 2 * i + 1];
    den += __shfl_xor(den, 32);
    den = rbf(rbf(den) + 1e-6f);
    const float inv = 1.f / den;
    u32x4 lo, hi;
#pragma unroll
    for (int i = 0; i < 4; ++i) {
      lo[i] = pack_bf16(rbf(num[2 * i]) * inv, rbf(num[2 * i + 1]) * inv);
      hi[i] = pack_bf16(rbf(num[8 + 2 * i]) * inv, rbf(num[9 + 2 * i]) * inv);
    }
    u16* dst = o + ((size_t)((rg * 8 + wave * 2 + m) * 64 + head * 2) * 512 + lane * 8);
    *(u32x4*)dst = lo;
    *(u32x4*)(dst + 512) = hi;
  }
}

constexpr int KD = 8;
constexpr int KS_LDS = 4 * 3 * 2 * 4096;
template <int KTOT, bool RES_FRAG, bool OUT_FRAG>
__global__ __launch_bounds__(256, 1) void gemm_ks_kernel(const u16* __restrict__ a, const u16* __restrict__ w, const u16* __restrict__ res,
                                                         u16* __restrict__ out) {
  extern __shared__ __attribute__((aligned(16))) char smem[];
  LAS char* lds = (LAS char*)smem;
  const int b = blockIdx.x, xcd = b & 7, idx = b >> 3;
  const int tm = (xcd >> 1) * 4 + (idx & 3), tn = (xcd & 1) * 8 + (idx >> 2);
  const int lane = threadIdx.x & 63, wave = __builtin_amdgcn_readfirstlane(threadIdx.x >> 6);
  constexpr int NS = KTOT / 64;
  static_assert(NS % KD == 0, "");
  const u32x4 sa = mk_srd(a + (size_t)tm * 128 * KTOT, 128u * KTOT * 2);
  const u32x4 sb = mk_srd(w, (unsigned)KTOT * D_ * 2);
  unsigned a_off[4];
#pragma unroll
  for (int m = 0; m < 4; ++m) a_off[m] = lane * 16 + m * (KTOT * 64) + wave * 1024;
  unsigned b_src;
  {
    const int row = lane >> 3, cs = lane & 7, c = cs ^ b_swz<128>(row);
    b_src = (unsigned)((wave * 16 + row) * D_ * 2 + tn * 128 + c * 16);
  }
  LAS char* ring = lds + wave * (KD * 2048);
  const unsigned ring0 = (unsigned)(size_t)ring;
  const int h = lane >> 5, l31 = lane & 31;
  const int grp = (lane >> 4) & 1, q = (lane & 15) >> 2, p = lane & 3, g = 4 * grp + p;
  const int pcol = 16 * (g >> 2) + 8 * (g & 1) + 4 * ((g >> 1) & 1);
  const int r0 = 8 * h + q, r1 = r0 + 4;
  const int b_rd0 = r0 * 128 + (((pcol >> 3) ^ b_swz<128>(r0)) << 4) + (pcol & 7) * 2;
  const int b_rd1 = r1 * 128 + (((pcol >> 3) ^ b_swz<128>(r1)) << 4) + (pcol & 7) * 2;

  f32x16 acc[4][2];
#pragma unroll
  for (int m = 0; m < 4; ++m)
#pragma unroll
    for (int n = 0; n < 2; ++n)
#pragma unroll
      for (int i = 0; i < 16; ++i) acc[m][n][i] = 0.f;
  bf16x8 ar[KD][4], bf[2][2];
  auto issue = [&](auto RC, int j) {
    constexpr int R = decltype(RC)::value;
    glds16(sb, ring0 + R * 2048, b_src, (unsigned)j * (64 * D_ * 2));
    glds16(sb, ring0 + R * 2048 + 1024, b_src, (unsigned)j * (64 * D_ * 2) + 8 * D_ * 2);
#pragma unroll
    for (int m = 0; m < 4; ++m) gld16<0>(ar[R][m], sa, a_off[m], (unsigned)j * 4096);
  };
  auto for_ring = [&](auto f) {
    f(std::integral_constant<int, 0>{}); f(std::integral_constant<int, 1>{}); f(std::integral_constant<int, 2>{}); f(std::integral_constant<int, 3>{});
    f(std::integral_constant<int, 4>{}); f(std::integral_constant<int, 5>{}); f(std::integral_constant<int, 6>{}); f(std::integral_constant<int, 7>{});
  };
  for_ring([&](auto RC) { issue(RC, decltype(RC)::value); });
  WAIT_V((KD - 1) * 6 + 4);
  bf[0][0] = tr_frag(ring + b_rd0, ring + b_rd1);
  bf[0][1] = tr_frag(ring + (b_rd0 ^ 64), ring + (b_rd1 ^ 64));
  SB();
#pragma unroll 1
  for (int j0 = 0; j0 < NS; j0 += KD) {
    for_ring([&](auto RC) {
      constexpr int R = decltype(RC)::value, cur = R & 1, nxt = cur ^ 1, RN = (R + 1) % KD;
      WAIT_V(4 + (KD - 2) * 6);
#pragma unroll
      for (int m = 0; m < 4; ++m) asm volatile("" : "+v"(ar[R][m]));
      SB();
      acc[0][0] = MFMA(bf[cur][0], ar[R][0], acc[0][0]);
      SB();
      bf[nxt][0] = tr_frag(ring + RN * 2048 + b_rd0, ring + RN * 2048 + b_rd1);
      SB();
      acc[1][0] = MFMA(bf[cur][0], ar[R][1], acc[1][0]);
      SB();
      bf[nxt][1] = tr_frag(ring + RN * 2048 + (b_rd0 ^ 64), ring + RN * 2048 + (b_rd1 ^ 64));
      SB();
      acc[2][0] = MFMA(bf[cur][0], ar[R][2], acc[2][0]);
      acc[3][0] = MFMA(bf[cur][0], ar[R][3], acc[3][0]);
      acc[0][1] = MFMA(bf[cur][1], ar[R][0], acc[0][1]);
      SB();
      glds16(sb, ring0 + R * 2048, b_src, (unsigned)(j0 + R + KD) * (64 * D_ * 2));
      SB();
      acc[1][1] = MFMA(bf[cur][1], ar[R][1], acc[1][1]);
      SB();
      glds16(sb, ring0 + R * 2048 + 1024, b_src, (unsigned)(j0 + R + KD) * (64 * D_ * 2) + 8 * D_ * 2);
      SB();
      acc[2][1] = MFMA(bf[cur][1], ar[R][2], acc[2][1]);
      acc[3][1] = MFMA(bf[cur][1], ar[R][3], acc[3][1]);
      SB();
#pragma unroll
      for (int m = 0; m < 4; ++m) gld16<0>(ar[R][m], sa, a_off[m], (unsigned)(j0 + R + KD) * 4096);
      SB();
    });
  }
  WAIT_V(0);
#pragma unroll
  for (int r = 0; r < KD; ++r)
#pragma unroll
    for (int m = 0; m < 4; ++m) asm volatile("" : "+v"(ar[r][m]));
  WAIT_L0();
  BAR();
  LAS f32x4* red = (LAS f32x4*)lds;
#pragma unroll
  for (int d = 1; d < 4; ++d) {
    const int dw = (wave + d) & 3;
#pragma unroll
    for (int m = 0; m < 4; ++m)
      if (m == dw) {
#pragma unroll
        for (int n = 0; n < 2; ++n)
#pragma unroll
          for (int c = 0; c < 4; ++c) {
            f32x4 v = {acc[m][n][4 * c], acc[m][n][4 * c + 1], acc[m][n][4 * c + 2], acc[m][n][4 * c + 3]};
            red[(((dw * 3 + (d - 1)) * 2 + n) * 4 + c) * 64 + lane] = v;
          }
      }
  }
  __syncthreads();
  f32x16 fin[2];
#pragma unroll
  for (int m = 0; m < 4; ++m)
    if (m == wave) { fin[0] = acc[m][0]; fin[1] = acc[m][1]; }
#pragma unroll
  for (int d = 0; d < 3; ++d)
#pragma unroll
    for (int n = 0; n < 2; ++n)
#pragma unroll
      for (int c = 0; c < 4; ++c) {
        const f32x4 v = red[(((wave * 3 + d) * 2 + n) * 4 + c) * 64 + lane];
#pragma unroll
        for (int e = 0; e < 4; ++e) fin[n][4 * c + e] += v[e];
      }
  const int mb = tm * 4 + wave;
#pragma unroll
  for (int n = 0; n < 2; ++n)
#pragma unroll
    for (int hf = 0; hf < 2; ++hf) {
      const int kb = tn * 4 + n * 2 + hf;
      const size_t foff = (size_t)(mb * 64 + kb) * 512 + lane * 8;
      const size_t roff = (size_t)(mb * 32 + l31) * D_ + kb * 16 + 8 * h;
      const u32x4 r = *(const u32x4*)(res + (RES_FRAG ? foff : roff));
      u32x4 o;
#pragma unroll
      for (int i = 0; i < 4; ++i)
        o[i] = pack_bf16(bf_lo(r[i]) + rbf(fin[n][8 * hf + 2 * i]), bf_hi(r[i]) + rbf(fin[n][8 * hf + 2 * i + 1]));
      *(u32x4*)(out + (OUT_FRAG ? foff : roff)) = o;
    }
}

__device__ __forceinline__ float gelu_tanh(float x) {
  const float u = x * (1.f + 0.044715f * x * x) * (-2.f * 0.7978845608028654f * 1.4426950408889634f);
  return x * __builtin_amdgcn_rcpf(1.f + __builtin_amdgcn_exp2f(u));
}
constexpr int ST3 = 4;
using SCfg3 = SCfg<3, 224, ST3>;
__global__ __launch_bounds__(256, 1) void gemm3_kernel(const u16* __restrict__ hn, const u16* __restrict__ wg, const u16* __restrict__ wu,
                                                       u16* __restrict__ g) {
  extern __shared__ __attribute__((aligned(16))) char smem[];
  LAS char* lds = (LAS char*)smem;
  const int b = blockIdx.x, xcd = b & 7, idx = b >> 3;
  const int tm = idx & 7, tn = xcd * 4 + (idx >> 3);
  f32x16 acc[2][7];
  gemm_shared<3, 2, 7, ST3, 1024>(acc, lds, hn + (size_t)tm * 256 * D_, wg, wu, F_, tn);
  const int lane = threadIdx.x & 63, wave = __builtin_amdgcn_readfirstlane(threadIdx.x >> 6);
#pragma unroll
  for (int m = 0; m < 2; ++m) {
    u16* dst = g + ((size_t)((tm * 8 + wave * 2 + m) * (F_ / 16) + tn * 7) * 512 + lane * 8);
#pragma unroll
    for (int n = 0; n < 7; ++n) {
      u32x4 o;
#pragma unroll
      for (int i = 0; i < 4; ++i) {
        const float g0 = rbf(gelu_tanh(rbf(acc[m][n][2 * i]))) * rbf(acc[m][n][8 + 2 * i]);
        const float g1 = rbf(gelu_tanh(rbf(acc[m][n][2 * i + 1]))) * rbf(acc[m][n][9 + 2 * i]);
        o[i] = pack_bf16(g0, g1);
      }
      *(u32x4*)(dst + n * 512) = o;
    }
  }
}

extern "C" void kernel_launch(void* const* d_in, const int* in_sizes, int n_in, void* d_out, int out_size, void* d_ws, size_t ws_size,
                              hipStream_t stream) {
  const u16* x = (const u16*)d_in[0];
  const u16* wqkv = (const u16*)d_in[1];
  const u16* wo = (const u16*)d_in[2];
  const u16* wg = (const u16*)d_in[4];
  const u16* wu = (const u16*)d_in[5];
  const u16* wd = (const u16*)d_in[6];
  u16* out = (u16*)d_out;
  char* ws = (char*)d_ws;
  const size_t MB = 1 << 20;
  u16* xn = (u16*)(ws);
  u16* pq = (u16*)(ws + 4 * MB);
  u16* o = (u16*)(ws + 8 * MB);
  u16* hbuf = (u16*)(ws + 12 * MB);
  u16* hn = (u16*)(ws + 16 * MB);
  float* kvp = (float*)(ws + 20 * MB);
  u16* g = (u16*)(ws + 28 * MB);

  (void)hipFuncSetAttribute((const void*)gemm1_kernel, hipFuncAttributeMaxDynamicSharedMemorySize, SCfg1::LDS_BYTES);
  (void)hipFuncSetAttribute((const void*)gemm3_kernel, hipFuncAttributeMaxDynamicSharedMemorySize, SCfg3::LDS_BYTES);
  (void)hipFuncSetAttribute((const void*)gemm_ks_kernel<1024, false, true>, hipFuncAttributeMaxDynamicSharedMemorySize, KS_LDS);
  (void)hipFuncSetAttribute((const void*)gemm_ks_kernel<3584, true, false>, hipFuncAttributeMaxDynamicSharedMemorySize, KS_LDS);

  ln_kernel<false><<<64, 512, 0, stream>>>(x, xn);
  gemm1_kernel<<<256, 256, SCfg1::LDS_BYTES, stream>>>(xn, wqkv, pq, kvp);
  o_kernel<<<256, 256, 0, stream>>>(pq, kvp, o);
  gemm_ks_kernel<1024, false, true><<<256, 256, KS_LDS, stream>>>(o, wo, x, hbuf);
  ln_kernel<true><<<64, 512, 0, stream>>>(hbuf, hn);
  gemm3_kernel<<<256, 256, SCfg3::LDS_BYTES, stream>>>(hn, wg, wu, g);
  gemm_ks_kernel<3584, true, false><<<256, 256, KS_LDS, stream>>>(g, wd, hbuf, out);
}
```

```cpp
#include <hip/hip_runtime.h>
#include <hip/hip_bf16.h>
#include <stdint.h>
#include <type_traits>
#include <utility>

#define LAS __attribute__((address_space(3)))
typedef unsigned short u16;
typedef __bf16 bf16x4 __attribute__((ext_vector_type(4)));
typedef __bf16 bf16x8 __attribute__((ext_vector_type(8)));
typedef float f32x4 __attribute__((ext_vector_type(4)));
typedef float f32x2 __attribute__((ext_vector_type(2)));
typedef unsigned u32x4 __attribute__((ext_vector_type(4)));
typedef unsigned u32x2 __attribute__((ext_vector_type(2)));
typedef __bf16 bf16x2 __attribute__((ext_vector_type(2)));

constexpr int S_ = 2048, D_ = 1024, F_ = 3584;

__device__ __forceinline__ unsigned pack_bf16(float lo, float hi) {
  bf16x2 v; v[0] = (__bf16)lo; v[1] = (__bf16)hi;
  return __builtin_bit_cast(unsigned, v);
}
__device__ __forceinline__ float bf_lo(unsigned u) { return __builtin_bit_cast(float, u << 16); }
__device__ __forceinline__ float bf_hi(unsigned u) { return __builtin_bit_cast(float, u & 0xffff0000u); }

#define WAIT_V(n) asm volatile("s_waitcnt vmcnt(%0)" ::"i"(n) : "memory")
#define WAIT_L0() asm volatile("s_waitcnt lgkmcnt(0)" ::: "memory")
#define BAR() asm volatile("s_barrier" ::: "memory")
#define SB() __builtin_amdgcn_sched_barrier(0)
template <class T> __device__ __forceinline__ void PIN(T& x) { asm volatile("" : "+v"(x)); }

#define TSDECL
#define TSARG
#define TSPASS
#define TSTAMP(s)
#define TSOUT(k, n)

template <int... I, class Fn>
__device__ __forceinline__ void sfor_impl(std::integer_sequence<int, I...>, Fn&& f) { (f(std::integral_constant<int, I>{}), ...); }
template <int N, class Fn>
__device__ __forceinline__ void sfor(Fn&& f) { sfor_impl(std::make_integer_sequence<int, N>{}, f); }

__device__ __forceinline__ u32x4 mk_srd(const void* p, unsigned bytes) {
  const unsigned long long a = (unsigned long long)p;
  u32x4 r;
  r[0] = __builtin_amdgcn_readfirstlane((unsigned)a);
  r[1] = __builtin_amdgcn_readfirstlane((unsigned)(a >> 32));
  r[2] = bytes;
  r[3] = 0x00020000u;
  return r;
}
__device__ __forceinline__ void glds16(const u32x4& srd, unsigned lds_dst, unsigned voff, unsigned soff) {
  asm volatile("s_mov_b32 m0, %0\n\ts_nop 0\n\tbuffer_load_dwordx4 %1, %2, %3 offen lds" ::"s"(lds_dst), "v"(voff), "s"(srd), "s"(soff) : "memory");
}
template <int IMM, class T>
__device__ __forceinline__ void gld16(T& dst, const u32x4& srd, unsigned voff, unsigned soff) {
  asm volatile("buffer_load_dwordx4 %0, %1, %2, %3 offen offset:%4" : "=v"(dst) : "v"(voff), "s"(srd), "s"(soff), "i"(IMM) : "memory");
}
template <int IMM, class T>
__device__ __forceinline__ void gld8(T& dst, const u32x4& srd, unsigned voff, unsigned soff) {
  asm volatile("buffer_load_dwordx2 %0, %1, %2, %3 offen offset:%4" : "=v"(dst) : "v"(voff), "s"(srd), "s"(soff), "i"(IMM) : "memory");
}
__device__ __forceinline__ bf16x8 tr_frag(LAS char* p0, LAS char* p1) {
  const bf16x4 lo = __builtin_amdgcn_ds_read_tr16_b64_v4bf16((LAS bf16x4*)p0);
  const bf16x4 hi = __builtin_amdgcn_ds_read_tr16_b64_v4bf16((LAS bf16x4*)p1);
  return __builtin_shufflevector(lo, hi, 0, 1, 2, 3, 4, 5, 6, 7);
}
#define MFMA(a, b, c) __builtin_amdgcn_mfma_f32_16x16x32_bf16(a, b, c, 0, 0, 0)
__device__ __forceinline__ void mfma_acc(f32x4& c, const bf16x8& a, const bf16x8& b) {
  asm volatile("v_mfma_f32_16x16x32_bf16 %0, %1, %2, %0" : "+a"(c) : "v"(a), "v"(b));
}
__device__ __forceinline__ void PINA(f32x4& c) { asm volatile("" : "+a"(c)); }

template <int MF, int KS, int BI, int ST, int AD, int XR, int PRO>
struct Sched {
  static constexpr int NS = 4 * AD, BP = (BI + KS - 1) / KS, MAXX = 64;
  int total[NS + 1] = {};
  int posA[NS + AD + 1] = {}, posB[NS + ST + 2] = {};
  int nx[AD] = {};
  int xkind[AD][MAXX] = {}, xidx[AD][MAXX] = {}, xpc[AD][MAXX] = {};
  int npro = 0, pkind[MAXX] = {}, pidx[MAXX] = {}, ppc[MAXX] = {};
  constexpr Sched() {
    int n = 0;
    int qk[512] = {}, qi[512] = {}, qp[512] = {}, nq = 0, hq = 0;
    for (int p = 0; p < BI; ++p) { qk[nq] = 1; qi[nq] = 0; qp[nq] = p; ++nq; }
    for (int a = 0; a < AD || (a + 1) / KS < ST; ++a) {
      if ((a + 1) % KS == 0 && (a + 1) / KS < ST) for (int p = 0; p < BI; ++p) { qk[nq] = 1; qi[nq] = (a + 1) / KS; qp[nq] = p; ++nq; }
      if (a < AD) for (int m = 0; m < MF; ++m) { qk[nq] = 0; qi[nq] = a; qp[nq] = m; ++nq; }
    }
    while (hq < nq && hq < PRO) {
      pkind[hq] = qk[hq]; pidx[hq] = qi[hq]; ppc[hq] = qp[hq];
      ++n; if (qk[hq]) posB[qi[hq]] = n; else posA[qi[hq]] = n;
      ++hq;
    }
    npro = hq;
    for (int j = 0; j < NS; ++j) {
      total[j] = n;
      if (j < AD) {
        int c = 0;
        while (hq < nq && (c < XR || j == AD - 1)) {
          xkind[j][c] = qk[hq]; xidx[j][c] = qi[hq]; xpc[j][c] = qp[hq];
          ++n; if (qk[hq]) posB[qi[hq]] = n; else posA[qi[hq]] = n;
          ++hq; ++c;
        }
        nx[j] = c;
      }
      if (j >= 1) { n += MF; posA[j - 1 + AD] = n; }
      if (j + 1 >= KS) {
        const int g = (j + 1) % KS, T = (j + 1) / KS - 1 + ST;
        for (int p = g * BP; p < (g + 1) * BP && p < BI; ++p) { ++n; posB[T] = n; }
      }
    }
    total[NS] = n;
  }
  constexpr int clampv(int v) const { return v > 63 ? 63 : v; }
  constexpr int waitA(int j) const { return clampv(total[j] - posA[j]); }
  constexpr int waitB(int j) const { return clampv(total[j] - posB[j / KS + 1]); }
  constexpr int min3(int a, int b, int c) const { return a < b ? (a < c ? a : c) : (b < c ? b : c); }
  constexpr int loopA(int r) const { return min3(waitA(AD + r), waitA(2 * AD + r), waitA(3 * AD + r)); }
  constexpr int loopB(int r) const { return min3(waitB(AD + r), waitB(2 * AD + r), waitB(3 * AD + r)); }
  constexpr bool ok() const {
    for (int j = 0; j < NS; ++j) {
      if (posA[j] <= 0 || posA[j] > total[j]) return false;
      if (j % KS == KS - 1 && (posB[j / KS + 1] <= 0 || posB[j / KS + 1] > total[j])) return false;
    }
    for (int j = 0; j < AD; ++j) if (nx[j] > MAXX) return false;
    if (posB[0] > total[0] || PRO > MAXX || 3 * AD / KS + 1 < ST) return false;
    return true;
  }
};

template <int MODE_, int MF_, int NF_, int KS_, int ST_, int AD_, int XR_, int PRO_, int KTOT_, int KLD_>
struct Cfg {
  static constexpr int MODE = MODE_, MF = MF_, NF = NF_, KS = KS_, ST = ST_, AD = AD_, XR = XR_, PRO = PRO_, KTOT = KTOT_, KLD = KLD_;
  static constexpr int BK = 32 * KS;
  static constexpr int ROWB = (MODE == 3) ? 224 : NF * 32;
  static constexpr int SUB = (MODE == 3) ? BK * ROWB : 0;
  static constexpr int STAGE = BK * NF * 32;
  static constexpr int BI = STAGE / 4096;
  static constexpr int LDS_BYTES = ST * STAGE;
  static_assert(STAGE % 4096 == 0 && AD % KS == 0 && AD % 2 == 0 && (KTOT / 32) % AD == 0, "");
};

template <class C> struct SchedOf {
  using SC = Sched<C::MF, C::KS, C::BI, C::ST, C::AD, C::XR, C::PRO>;
  static constexpr SC S{};
};
template <class C> __device__ __forceinline__ int b_swz(int row) {
  if (C::MODE == 3) return 0;
  if (C::MODE == 1) return ((row >> 1) & 1) | (((row >> 3) & 1) << 1);
  if (C::ROWB % 256 == 0) return row & 7;
  return (row >> 1) & 3;
}

template <class C, class Mid>
__device__ __forceinline__ void gemm_main(f32x4 (&acc)[C::MF][C::NF], LAS char* lds, const u16* A, const u16* B0, const u16* B1, unsigned b_bytes,
                                          int ldb, Mid&& mid TSARG) {
  constexpr int MF = C::MF, NF = C::NF, KS = C::KS, ST = C::ST, AD = C::AD, BI = C::BI, ROWB = C::ROWB, NMM = MF * NF;
  using SO = SchedOf<C>;
  static_assert(SO::S.ok(), "schedule");
  constexpr int BP = SO::SC::BP;
  TSTAMP(0);
  const int lane = threadIdx.x & 63;
  const int wave = __builtin_amdgcn_readfirstlane(threadIdx.x >> 6);
  const unsigned lds0 = (unsigned)(size_t)lds;
  const u32x4 sa = mk_srd(A + (size_t)wave * MF * 16 * C::KLD, (unsigned)((MF - 1) * 16 * C::KLD + 16 * C::KTOT) * 2);
  const u32x4 sb = mk_srd((C::MODE == 3 && wave >= 2) ? B1 : B0, b_bytes);
  unsigned a_off[MF];
#pragma unroll
  for (int m = 0; m < MF; ++m) a_off[m] = lane * 16 + m * (C::KLD * 32);
  unsigned b_src[BI];
#pragma unroll
  for (int i = 0; i < BI; ++i) {
    if (C::MODE == 3) {
      const int ch = ((wave & 1) * BI + i) * 64 + lane, row = ch / 14, c = ch % 14;
      b_src[i] = (unsigned)(row * ldb * 2 + c * 16);
    } else {
      constexpr int CPR = ROWB / 16;
      const int ch = (wave * BI + i) * 64 + lane, row = ch / CPR, cs = ch % CPR, c = cs ^ (b_swz<C>(row) << 1);
      const int col = (C::MODE == 1) ? (c >> 3) * 1024 + (c & 7) * 8 : c * 8;
      b_src[i] = (unsigned)(row * ldb * 2 + col * 2);
    }
  }
  const unsigned b_dst = lds0 + wave * BI * 1024;
  const unsigned b_tile = (unsigned)(C::BK * ldb * 2);

  const int G = lane >> 4, kr = (lane & 15) >> 2, p = lane & 3;
  const int r0 = (C::MODE == 1 ? 8 : 4) * G + kr;
  constexpr int RD2 = (C::MODE == 1 ? 4 : 16) * ROWB;
  constexpr int NV = (C::MODE == 3) ? 1 : (ROWB % 256 == 0 ? 8 : 4);
  int b_rd[NV];
#pragma unroll
  for (int v = 0; v < NV; ++v) b_rd[v] = r0 * ROWB + ((v ^ b_swz<C>(r0)) << 5) + p * 8;
  auto half = [&](LAS char* base, int n, int s, int hf) -> bf16x4 {
    int off;
    if (C::MODE == 3) off = b_rd[0] + (n & 1) * C::SUB + (n >> 1) * 32;
    else off = b_rd[n % NV] + (n / NV) * (NV * 32);
    off += s * 32 * ROWB + hf * RD2;
    return __builtin_amdgcn_ds_read_tr16_b64_v4bf16((LAS bf16x4*)(base + off));
  };

  bf16x8 ar[AD][MF];
  bf16x4 bf[2][NF][2];
  auto issueA = [&](auto RC, auto MC, unsigned step) { gld16<0>(ar[decltype(RC)::value][decltype(MC)::value], sa, a_off[decltype(MC)::value], step < C::KTOT / 32 ? step * 1024 : 0x7ff00000u); };
  sfor<SO::S.npro>([&](auto LC) {
    constexpr int l = decltype(LC)::value, kind = SO::S.pkind[l], idx = SO::S.pidx[l], pc = SO::S.ppc[l];
    if constexpr (kind) glds16(sb, b_dst + idx * C::STAGE + pc * 1024, b_src[pc], idx * b_tile);
    else issueA(std::integral_constant<int, idx>{}, std::integral_constant<int, pc>{}, idx);
  });
#pragma unroll
  for (int m = 0; m < MF; ++m)
#pragma unroll
    for (int n = 0; n < NF; ++n) { acc[m][n] = f32x4{0.f, 0.f, 0.f, 0.f}; PINA(acc[m][n]); }
  TSTAMP(1);
  { constexpr int w0 = SO::S.total[0] - SO::S.posB[0]; WAIT_V(w0); }
  BAR();
  TSTAMP(2);
#pragma unroll
  for (int n = 0; n < NF; ++n) { bf[0][n][0] = half(lds, n, 0, 0); bf[0][n][1] = half(lds, n, 0, 1); }
  SB();

  int slot = 0, fslot = 0;
  auto step = [&](auto JC, auto FC, int j0) {
    constexpr int R = decltype(JC)::value;
    constexpr bool FIRST = decltype(FC)::value;
    constexpr int s = R % KS, cur = R & 1, nxt = cur ^ 1;
    constexpr int WA = FIRST ? SO::S.waitA(R) : SO::S.loopA(R), WB = (R % KS != KS - 1) ? 63 : (FIRST ? SO::S.waitB(R) : SO::S.loopB(R));
    WAIT_V(WA);
    sfor<MF>([&](auto m) { PIN(ar[R][decltype(m)::value]); });
    if constexpr (s == KS - 1) {
      WAIT_L0();
      if constexpr (WB < WA) WAIT_V(WB);
      BAR();
      fslot = slot;
      slot = (slot + 1 == ST) ? 0 : slot + 1;
    }
    LAS char* rbase = lds + slot * C::STAGE;
    constexpr int ns = (s + 1) % KS;
    constexpr bool HASB = !(FIRST && R + 1 < KS);
    constexpr int g = (R + 1) % KS;
    constexpr int NB = HASB ? ((g + 1) * BP <= BI ? BP : (BI - g * BP > 0 ? BI - g * BP : 0)) : 0;
    constexpr int NX = FIRST ? SO::S.nx[R] : 0;
    constexpr int NA = (FIRST && R == 0) ? 0 : MF, RP = (R + AD - 1) % AD;
    constexpr int NL = NX + NA + NB;
    const unsigned gdst = b_dst + fslot * C::STAGE;
    const unsigned gsoff = (unsigned)((j0 + R + 1) / KS - 1 + ST) * b_tile;
    SB();
    sfor<NMM>([&](auto IC) {
      constexpr int i = decltype(IC)::value, n = i / MF, m = i % MF;
      const bf16x8 bb = __builtin_shufflevector(bf[cur][n][0], bf[cur][n][1], 0, 1, 2, 3, 4, 5, 6, 7);
      if constexpr (C::MODE == 1 && n >= 4) mfma_acc(acc[m][n], ar[R][m], bb);
      else mfma_acc(acc[m][n], bb, ar[R][m]);
      SB();
      constexpr int SPAN = (NMM * 7) / 8 >= 2 * NF ? (NMM * 7) / 8 : NMM;
      sfor<2 * NF>([&](auto NC) {
        constexpr int nn = decltype(NC)::value;
        if constexpr ((nn * SPAN) / (2 * NF) == i) bf[nxt][nn >> 1][nn & 1] = half(rbase, nn >> 1, ns, nn & 1);
      });
      sfor<NL>([&](auto LC) {
        constexpr int l = decltype(LC)::value;
        if constexpr (((2 * l + 1) * NMM) / (2 * NL) == i) {
          if constexpr (l < NX) {
            constexpr int kind = SO::S.xkind[R][l], idx = SO::S.xidx[R][l], pc = SO::S.xpc[R][l];
            if constexpr (kind) glds16(sb, b_dst + idx * C::STAGE + pc * 1024, b_src[pc], idx * b_tile);
            else issueA(std::integral_constant<int, idx>{}, std::integral_constant<int, pc>{}, idx);
          } else if constexpr (l < NX + NA) {
            issueA(std::integral_constant<int, RP>{}, std::integral_constant<int, l - NX>{}, (unsigned)(j0 + R - 1 + AD));
          } else {
            constexpr int pc = g * BP + (l - NX - NA);
            glds16(sb, gdst + pc * 1024, b_src[pc], gsoff);
          }
        }
      });
      SB();
    });
  };
  sfor<AD>([&](auto r) { step(r, std::true_type{}, 0); });
  mid();
  SB();
#pragma unroll 1
  for (int j0 = AD; j0 < C::KTOT / 32; j0 += AD) sfor<AD>([&](auto r) { step(r, std::false_type{}, j0); });
  asm volatile("s_nop 15\n\ts_nop 15" ::: "memory");
#pragma unroll
  for (int m = 0; m < MF; ++m)
#pragma unroll
    for (int n = 0; n < NF; ++n) PINA(acc[m][n]);
  TSTAMP(3);
  WAIT_V(0);
  sfor<AD>([&](auto r) { sfor<MF>([&](auto m) { PIN(ar[decltype(r)::value][decltype(m)::value]); }); });
  WAIT_L0();
  TSTAMP(5);
}
#define C1_ST 4
#define C1_AD 4
#define C1_XR 6
#define C2_ST 4
#define C2_XR 3
#define C3_ST 4
#define C3_XR 10
#define C4_ST 4
#define C4_XR 3
#define C1_PRO 16
#define C2_PRO 16
#define C3_PRO 18
#define C4_PRO 16
#define MAP4 0

__global__ __launch_bounds__(256) void ln1_kernel(const u16* __restrict__ x, u16* __restrict__ y) {
  __shared__ float red[2][4][8];
  const int t = threadIdx.x, wave = t >> 6, r = t & 7, c0 = t >> 3, row = blockIdx.x * 8 + r;
  u32x4 v[4];
#pragma unroll
  for (int i = 0; i < 4; ++i) v[i] = *(const u32x4*)(x + (size_t)row * D_ + (c0 + 32 * i) * 8);
  float s = 0.f;
#pragma unroll
  for (int i = 0; i < 4; ++i)
#pragma unroll
    for (int j = 0; j < 4; ++j) s += bf_lo(v[i][j]) + bf_hi(v[i][j]);
  s += __shfl_xor(s, 8); s += __shfl_xor(s, 16); s += __shfl_xor(s, 32);
  if ((t & 63) < 8) red[0][wave][r] = s;
  __syncthreads();
  const float m = (red[0][0][r] + red[0][1][r] + red[0][2][r] + red[0][3][r]) * (1.f / D_);
  float q = 0.f;
#pragma unroll
  for (int i = 0; i < 4; ++i)
#pragma unroll
    for (int j = 0; j < 4; ++j) { const float a = bf_lo(v[i][j]) - m, b = bf_hi(v[i][j]) - m; q += a * a + b * b; }
  q += __shfl_xor(q, 8); q += __shfl_xor(q, 16); q += __shfl_xor(q, 32);
  if ((t & 63) < 8) red[1][wave][r] = q;
  __syncthreads();
  const float var = (red[1][0][r] + red[1][1][r] + red[1][2][r] + red[1][3][r]) * (1.f / D_);
  const float rs = 1.f / sqrtf(var + 1e-5f);
#pragma unroll
  for (int i = 0; i < 4; ++i) {
    u32x4 o;
#pragma unroll
    for (int j = 0; j < 4; ++j) o[j] = pack_bf16((bf_lo(v[i][j]) - m) * rs, (bf_hi(v[i][j]) - m) * rs);
    const int c = c0 + 32 * i;
    *(u32x4*)(y + ((size_t)((row >> 4) * 32 + (c >> 2)) * 64 + (c & 3) * 16 + (row & 15)) * 8) = o;
  }
}

__device__ __forceinline__ float phi(float t) {
  const float e = __builtin_amdgcn_fmed3f(__builtin_amdgcn_exp2f(t * 1.4426950408889634f), 0.f, 1.f);
  return (fmaxf(t, 0.f) + e) + 1e-6f;
}
__device__ __forceinline__ bf16x8 pack8(const f32x4& a, const f32x4& b) {
  u32x4 o = {pack_bf16(a[0], a[1]), pack_bf16(a[2], a[3]), pack_bf16(b[0], b[1]), pack_bf16(b[2], b[3])};
  return __builtin_bit_cast(bf16x8, o);
}
__device__ __forceinline__ f32x4 phi4(const f32x4& a) { return f32x4{phi(a[0]), phi(a[1]), phi(a[2]), phi(a[3])}; }

constexpr int KVP = 1024 + 32;
using C1 = Cfg<1, 2, 12, 2, C1_ST, C1_AD, C1_XR, C1_PRO, 1024, 1024>;
__global__ __launch_bounds__(256, 1) void gemm1_kernel(const u16* __restrict__ xn, const u16* __restrict__ wqkv, u16* __restrict__ pq,
                                                       float* __restrict__ kvp) {
  extern __shared__ __attribute__((aligned(16))) char smem[];
  LAS char* lds = (LAS char*)smem;
  const int b = blockIdx.x, xcd = b & 7, idx = b >> 3;
  const int tm = (xcd >> 2) * 8 + (idx & 7), tn = (xcd & 3) * 4 + (idx >> 3);
  f32x4 acc[2][12];
  TSDECL;
  gemm_main<C1>(acc, lds, xn + (size_t)tm * 128 * D_, wqkv + tn * 64, wqkv, (unsigned)(D_ * 3 * D_ - tn * 64) * 2, 3 * D_, [] {} TSPASS);
  const int lane = threadIdx.x & 63, wave = __builtin_amdgcn_readfirstlane(threadIdx.x >> 6);
#pragma unroll
  for (int m = 0; m < 2; ++m)
#pragma unroll
    for (int hh = 0; hh < 2; ++hh)
      *(bf16x8*)(pq + ((size_t)((tm * 8 + wave * 2 + m) * 32 + tn * 2 + hh) * 64 + lane) * 8) = pack8(phi4(acc[m][2 * hh]), phi4(acc[m][2 * hh + 1]));
  BAR();
  LAS f32x4* red = (LAS f32x4*)lds;
  const u32x4 one4 = {0x3f803f80u, 0x3f803f80u, 0x3f803f80u, 0x3f803f80u};
  const f32x4 z4 = {0.f, 0.f, 0.f, 0.f};
#pragma unroll
  for (int hh = 0; hh < 2; ++hh) {
    const bf16x8 v0 = pack8(acc[0][8 + 2 * hh], acc[1][8 + 2 * hh]), v1 = pack8(acc[0][9 + 2 * hh], acc[1][9 + 2 * hh]);
#pragma unroll
    for (int i = 0; i < 2; ++i) {
      const bf16x8 pk = pack8(phi4(acc[0][4 + 2 * hh + i]), phi4(acc[1][4 + 2 * hh + i]));
      red[((wave * 2 + hh) * 6 + 2 * i) * 64 + lane] = MFMA(pk, v0, z4);
      red[((wave * 2 + hh) * 6 + 2 * i + 1) * 64 + lane] = MFMA(pk, v1, z4);
      red[((wave * 2 + hh) * 6 + 4 + i) * 64 + lane] = MFMA(pk, __builtin_bit_cast(bf16x8, one4), z4);
    }
  }
  __syncthreads();
#pragma unroll
  for (int e = 0; e < 3; ++e) {
    const int blk = e * 4 + wave, hh = blk / 6, bb = blk % 6;
    const f32x4 s = (red[blk * 64 + lane] + red[(12 + blk) * 64 + lane]) + (red[(24 + blk) * 64 + lane] + red[(36 + blk) * 64 + lane]);
    float* dst = kvp + ((size_t)(tn * 2 + hh) * 16 + tm) * KVP;
    if (bb < 4) *(f32x4*)(dst + bb * 256 + lane * 4) = s;
    else if ((lane & 15) == 0) *(f32x4*)(dst + 1024 + (bb - 4) * 16 + (lane >> 4) * 4) = s;
  }
  TSTAMP(4);
  TSOUT(0, 6);
}

__global__ __launch_bounds__(256) void o_kernel(const u16* __restrict__ pq, const float* __restrict__ kvp, u16* __restrict__ o,
                                                const u16* __restrict__ wg, const u16* __restrict__ wu, float* __restrict__ cs) {
  __shared__ __attribute__((aligned(16))) float sh[2048];
  const int t = threadIdx.x, lane = t & 63, wave = t >> 6;
  if (blockIdx.x >= 256) {
    const int cb = blockIdx.x - 256;
    const u16* w = (cb >= 112 ? wu : wg) + (cb % 112) * 32 + (t & 3) * 8 + (size_t)(t >> 2) * F_;
    u32x4 v[16];
#pragma unroll
    for (int i = 0; i < 16; ++i) v[i] = *(const u32x4*)(w + (size_t)i * 64 * F_);
    float s[8];
#pragma unroll
    for (int j = 0; j < 8; ++j) s[j] = 0.f;
#pragma unroll
    for (int i = 0; i < 16; ++i)
#pragma unroll
      for (int j = 0; j < 4; ++j) { s[2 * j] += bf_lo(v[i][j]); s[2 * j + 1] += bf_hi(v[i][j]); }
#pragma unroll
    for (int j = 0; j < 8; ++j) sh[(t >> 2) * 32 + (t & 3) * 8 + j] = s[j];
    __syncthreads();
    float a = 0.f;
#pragma unroll
    for (int i = 0; i < 8; ++i) a += sh[((t >> 5) * 8 + i) * 32 + (t & 31)];
    __syncthreads();
    sh[t] = a;
    __syncthreads();
    if (t < 32) {
      float r = 0.f;
#pragma unroll
      for (int i = 0; i < 8; ++i) r += sh[i * 32 + t];
      cs[cb * 32 + t] = r;
    }
    return;
  }
  const int head = blockIdx.x & 31, rg = blockIdx.x >> 5;
  const float* src = kvp + (size_t)head * 16 * KVP;
  f32x4 pv[16];
#pragma unroll
  for (int i = 0; i < 16; ++i) pv[i] = *(const f32x4*)(src + i * KVP + t * 4);
  f32x4 zp = {0.f, 0.f, 0.f, 0.f};
  if (t < 128) zp = *(const f32x4*)(src + (t >> 3) * KVP + 1024 + (t & 7) * 4);
  bf16x8 qf[4];
#pragma unroll
  for (int m = 0; m < 4; ++m) qf[m] = *(const bf16x8*)(pq + ((size_t)((rg * 16 + wave * 4 + m) * 32 + head) * 64 + lane) * 8);
#pragma unroll
  for (int st = 8; st > 0; st >>= 1)
#pragma unroll
    for (int i = 0; i < st; ++i) pv[i] += pv[i + st];
  u32x2 pk2 = {pack_bf16(pv[0][0], pv[0][1]), pack_bf16(pv[0][2], pv[0][3])};
  *(u32x2*)((char*)sh + ((wave & 1) * 64 + lane) * 16 + (wave >> 1) * 8) = pk2;
  if (t < 128) *(f32x4*)(sh + 512 + t * 4) = zp;
  __syncthreads();
  const bf16x8 kv0 = *(const bf16x8*)((char*)sh + lane * 16), kv1 = *(const bf16x8*)((char*)sh + (64 + lane) * 16);
  f32x4 za = {0.f, 0.f, 0.f, 0.f}, zb = za;
#pragma unroll
  for (int i = 0; i < 16; ++i) {
    za += *(const f32x4*)(sh + 512 + i * 32 + (lane >> 4) * 4);
    zb += *(const f32x4*)(sh + 512 + i * 32 + 16 + (lane >> 4) * 4);
  }
  const bf16x8 zf = pack8(za, zb);
  const f32x4 z4 = {0.f, 0.f, 0.f, 0.f};
#pragma unroll
  for (int m = 0; m < 4; ++m) {
    const f32x4 n0 = MFMA(kv0, qf[m], z4), n1 = MFMA(kv1, qf[m], z4), dn = MFMA(zf, qf[m], z4);
    const float inv = 1.f / (dn[0] + 1e-6f);
    *(bf16x8*)(o + ((size_t)((rg * 16 + wave * 4 + m) * 32 + head) * 64 + lane) * 8) = pack8(n0 * inv, n1 * inv);
  }
}

using C2 = Cfg<2, 2, 4, 4, C2_ST, 8, C2_XR, C2_PRO, 1024, 1024>;
__global__ __launch_bounds__(256, 1) void gemm2_kernel(const u16* __restrict__ o, const u16* __restrict__ wo, const u16* __restrict__ x,
                                                       u16* __restrict__ h, float* __restrict__ stats) {
  extern __shared__ __attribute__((aligned(16))) char smem[];
  LAS char* lds = (LAS char*)smem;
  const int b = blockIdx.x, xcd = b & 7, idx = b >> 3;
  const int tm = (xcd >> 1) * 4 + (idx & 3), tn = (xcd & 1) * 8 + (idx >> 2);
  const int lane = threadIdx.x & 63, wave = __builtin_amdgcn_readfirstlane(threadIdx.x >> 6);
  f32x4 acc[2][4];
  u32x2 res[2][4];
  const u32x4 sx = mk_srd(x + (size_t)(tm * 128 + wave * 32) * D_ + tn * 64, 32 * D_ * 2);
  const unsigned xo = (unsigned)((lane & 15) * D_ * 2 + (lane >> 4) * 8);
  TSDECL;
  gemm_main<C2>(acc, lds, o + (size_t)tm * 128 * D_, wo + tn * 64, wo, (unsigned)(D_ * D_ - tn * 64) * 2, D_, [&] {
    sfor<2>([&](auto m) { sfor<4>([&](auto n) { gld8<decltype(n)::value * 32>(res[decltype(m)::value][decltype(n)::value], sx, xo, decltype(m)::value * 16 * D_ * 2); }); });
  } TSPASS);
#pragma unroll
  for (int m = 0; m < 2; ++m) {
    unsigned w[8];
    float s = 0.f;
#pragma unroll
    for (int n = 0; n < 4; ++n) {
      PIN(res[m][n]);
      w[2 * n] = pack_bf16(bf_lo(res[m][n][0]) + acc[m][n][0], bf_hi(res[m][n][0]) + acc[m][n][1]);
      w[2 * n + 1] = pack_bf16(bf_lo(res[m][n][1]) + acc[m][n][2], bf_hi(res[m][n][1]) + acc[m][n][3]);
    }
    const int mb = tm * 8 + wave * 2 + m;
    *(u32x4*)(h + ((size_t)(mb * 32 + tn * 2) * 64 + lane) * 8) = u32x4{w[0], w[1], w[2], w[3]};
    *(u32x4*)(h + ((size_t)(mb * 32 + tn * 2 + 1) * 64 + lane) * 8) = u32x4{w[4], w[5], w[6], w[7]};
#pragma unroll
    for (int i = 0; i < 8; ++i) s += bf_lo(w[i]) + bf_hi(w[i]);
    s += __shfl_xor(s, 16); s += __shfl_xor(s, 32);
    const float mu = s * (1.f / 64.f);
    float q = 0.f;
#pragma unroll
    for (int i = 0; i < 8; ++i) { const float a = bf_lo(w[i]) - mu, c = bf_hi(w[i]) - mu; q += a * a + c * c; }
    q += __shfl_xor(q, 16); q += __shfl_xor(q, 32);
    if (lane < 16) *(f32x2*)(stats + ((size_t)(mb * 16 + lane) * 16 + tn) * 2) = f32x2{s, q};
  }
  TSTAMP(4);
  TSOUT(1, 6);
}

using C3 = Cfg<3, 4, 14, 2, C3_ST, 4, C3_XR, C3_PRO, 1024, 1024>;
constexpr int LDS3 = C3::LDS_BYTES + 1024;
template <int PAR>
__device__ __forceinline__ void geglu_store(f32x4 (&acc)[4][14], const float (&mu)[4], const float (&var)[4], LAS char* csl, u16* g, int mb0, int tn,
                                            int lane) {
  const int q = lane >> 4;
#pragma unroll
  for (int m = 0; m < 4; ++m) {
    const float ir2 = var[m], r = __builtin_amdgcn_rsqf(ir2), k1 = -2.f * 0.7978845608028654f * 1.4426950408889634f * r, k2 = k1 * 0.044715f * r * r;
    char* dst = (char*)g + ((size_t)(mb0 + m) * (F_ / 32) + ((tn * 7) >> 1)) * 1024;
    unsigned w[14];
#pragma unroll
    for (int n = 0; n < 7; ++n) {
      const f32x4 cg = *(const LAS f32x4*)(csl + n * 64 + q * 16), cu = *(const LAS f32x4*)(csl + 448 + n * 64 + q * 16);
      const f32x4 a = acc[m][2 * n] - mu[m] * cg, u = acc[m][2 * n + 1] - mu[m] * cu;
      const f32x4 arg = a * (a * a * k2 + k1);
      f32x4 d;
#pragma unroll
      for (int i = 0; i < 4; ++i) d[i] = __builtin_amdgcn_exp2f(arg[i]);
      d = d * ir2 + ir2;
#pragma unroll
      for (int i = 0; i < 4; ++i) d[i] = __builtin_amdgcn_rcpf(d[i]);
      const f32x4 o = a * u * d;
      w[2 * n] = pack_bf16(o[0], o[1]);
      w[2 * n + 1] = pack_bf16(o[2], o[3]);
    }
    if (PAR == 0) {
#pragma unroll
      for (int i = 0; i < 3; ++i) *(u32x4*)(dst + i * 1024 + lane * 16) = u32x4{w[4 * i], w[4 * i + 1], w[4 * i + 2], w[4 * i + 3]};
      *(u32x2*)(dst + 3 * 1024 + lane * 16) = u32x2{w[12], w[13]};
    } else {
      *(u32x2*)(dst + lane * 16 + 8) = u32x2{w[0], w[1]};
#pragma unroll
      for (int i = 0; i < 3; ++i) *(u32x4*)(dst + (i + 1) * 1024 + lane * 16) = u32x4{w[4 * i + 2], w[4 * i + 3], w[4 * i + 4], w[4 * i + 5]};
    }
  }
}
__global__ __launch_bounds__(256, 1) void gemm3_kernel(const u16* __restrict__ h, const u16* __restrict__ wg, const u16* __restrict__ wu,
                                                       const float* __restrict__ stats, const float* __restrict__ cs, u16* __restrict__ g) {
  extern __shared__ __attribute__((aligned(16))) char smem[];
  LAS char* lds = (LAS char*)smem;
  const int b = blockIdx.x, xcd = b & 7, idx = b >> 3;
  const int tm = idx & 7, tn = xcd * 4 + (idx >> 3);
  const int lane = threadIdx.x & 63, wave = __builtin_amdgcn_readfirstlane(threadIdx.x >> 6);
  f32x4 acc[4][14];
  f32x4 st[8];
  const u32x4 ss = mk_srd(stats + (size_t)(tm * 256 + wave * 64) * 32, 64 * 128);
  const u32x4 sc = mk_srd(cs, 2 * F_ * 4);
  const unsigned co = lane < 56 ? (unsigned)((lane >= 28 ? F_ * 4 - 448 : 0) + tn * 448 + lane * 16) : 0x7ffffff0u;
  TSDECL;
  gemm_main<C3>(acc, lds, h + (size_t)tm * 256 * D_, wg + tn * 112, wu + tn * 112, (unsigned)(D_ * F_ - tn * 112) * 2, F_, [&] {
    sfor<8>([&](auto i) { gld16<decltype(i)::value * 16>(st[decltype(i)::value], ss, lane * 128, 0); });
    if (wave == 0) glds16(sc, (unsigned)(size_t)lds + C3::LDS_BYTES, co, 0);
  } TSPASS);
  float mu[4], var[4];
  {
    float s = 0.f, q = 0.f, dd = 0.f;
#pragma unroll
    for (int i = 0; i < 8; ++i) { PIN(st[i]); s += st[i][0] + st[i][2]; q += st[i][1] + st[i][3]; }
    const float mean = s * (1.f / D_);
#pragma unroll
    for (int i = 0; i < 8; ++i) {
      const float d0 = st[i][0] * (1.f / 64.f) - mean, d1 = st[i][2] * (1.f / 64.f) - mean;
      dd += d0 * d0 + d1 * d1;
    }
    const float v = (q + 64.f * dd) * (1.f / D_) + 1e-5f;
#pragma unroll
    for (int m = 0; m < 4; ++m) { mu[m] = __shfl(mean, m * 16 + (lane & 15)); var[m] = __shfl(v, m * 16 + (lane & 15)); }
  }
  TSTAMP(6);
  if (tn & 1) geglu_store<1>(acc, mu, var, lds + C3::LDS_BYTES, g, tm * 16 + wave * 4, tn, lane);
  else geglu_store<0>(acc, mu, var, lds + C3::LDS_BYTES, g, tm * 16 + wave * 4, tn, lane);
  TSTAMP(4);
  TSOUT(2, 7);
}

using C4 = Cfg<2, 2, 4, 4, C4_ST, 8, C4_XR, C4_PRO, F_, F_>;
__global__ __launch_bounds__(256, 1) void gemm4_kernel(const u16* __restrict__ g, const u16* __restrict__ wd, const u16* __restrict__ h,
                                                       u16* __restrict__ out) {
  extern __shared__ __attribute__((aligned(16))) char smem[];
  LAS char* lds = (LAS char*)smem;
  const int b = blockIdx.x, xcd = b & 7, idx = b >> 3;
  const int tm = (xcd >> 1) * 4 + (idx & 3), tn = (xcd & 1) * 8 + (idx >> 2);
  const int lane = threadIdx.x & 63, wave = __builtin_amdgcn_readfirstlane(threadIdx.x >> 6);
  f32x4 acc[2][4];
  u32x4 res[2][2];
  const u32x4 sh = mk_srd(h + ((size_t)(tm * 8 + wave * 2) * 32 + tn * 2) * 512, 34 * 1024);
  TSDECL;
  gemm_main<C4>(acc, lds, g + (size_t)tm * 128 * F_, wd + tn * 64, wd, (unsigned)(F_ * D_ - tn * 64) * 2, D_, [&] {
    sfor<2>([&](auto m) { gld16<0>(res[decltype(m)::value][0], sh, lane * 16, decltype(m)::value * 32768); gld16<1024>(res[decltype(m)::value][1], sh, lane * 16, decltype(m)::value * 32768); });
  } TSPASS);
  BAR();
  LAS char* patch = lds + wave * (32 * 144);
#pragma unroll
  for (int m = 0; m < 2; ++m)
#pragma unroll
    for (int n = 0; n < 4; ++n) {
      PIN(res[m][n >> 1]);
      const unsigned r0 = res[m][n >> 1][(n & 1) * 2], r1 = res[m][n >> 1][(n & 1) * 2 + 1];
      const u32x2 w = {pack_bf16(bf_lo(r0) + acc[m][n][0], bf_hi(r0) + acc[m][n][1]), pack_bf16(bf_lo(r1) + acc[m][n][2], bf_hi(r1) + acc[m][n][3])};
      *(LAS u32x2*)(patch + (m * 16 + (lane & 15)) * 144 + n * 32 + (lane >> 4) * 8) = w;
    }
#pragma unroll
  for (int i = 0; i < 4; ++i) {
    const int row = i * 8 + (lane >> 3);
    const u32x4 w = *(const LAS u32x4*)(patch + row * 144 + (lane & 7) * 16);
    *(u32x4*)(out + (size_t)(tm * 128 + wave * 32 + row) * D_ + tn * 64 + (lane & 7) * 8) = w;
  }
  TSTAMP(4);
  TSOUT(3, 6);
}

extern "C" void kernel_launch(void* const* d_in, const int* in_sizes, int n_in, void* d_out, int out_size, void* d_ws, size_t ws_size,
                              hipStream_t stream) {
  const u16* x = (const u16*)d_in[0];
  const u16* wqkv = (const u16*)d_in[1];
  const u16* wo = (const u16*)d_in[2];
  const u16* wg = (const u16*)d_in[4];
  const u16* wu = (const u16*)d_in[5];
  const u16* wd = (const u16*)d_in[6];
  u16* out = (u16*)d_out;
  char* ws = (char*)d_ws;
  const size_t MB = 1 << 20;
  u16* xn = (u16*)(ws);
  u16* pq = (u16*)(ws + 4 * MB);
  u16* o = (u16*)(ws + 8 * MB);
  u16* hbuf = (u16*)(ws + 12 * MB);
  float* kvp = (float*)(ws + 16 * MB);
  float* stats = (float*)(ws + 20 * MB);
  float* cs = (float*)(ws + 21 * MB);
  u16* g = (u16*)(ws + 24 * MB);

  (void)hipFuncSetAttribute((const void*)gemm1_kernel, hipFuncAttributeMaxDynamicSharedMemorySize, C1::LDS_BYTES);
  (void)hipFuncSetAttribute((const void*)gemm2_kernel, hipFuncAttributeMaxDynamicSharedMemorySize, C2::LDS_BYTES);
  (void)hipFuncSetAttribute((const void*)gemm3_kernel, hipFuncAttributeMaxDynamicSharedMemorySize, LDS3);
  (void)hipFuncSetAttribute((const void*)gemm4_kernel, hipFuncAttributeMaxDynamicSharedMemorySize, C4::LDS_BYTES);

  ln1_kernel<<<256, 256, 0, stream>>>(x, xn);
  gemm1_kernel<<<256, 256, C1::LDS_BYTES, stream>>>(xn, wqkv, pq, kvp);
  o_kernel<<<256 + 224, 256, 0, stream>>>(pq, kvp, o, wg, wu, cs);
  gemm2_kernel<<<256, 256, C2::LDS_BYTES, stream>>>(o, wo, x, hbuf, stats);
  gemm3_kernel<<<256, 256, LDS3, stream>>>(hbuf, wg, wu, stats, cs, g);
  gemm4_kernel<<<256, 256, C4::LDS_BYTES, stream>>>(g, wd, hbuf, out);
}
```

```cpp
#include <hip/hip_runtime.h>
#include <hip/hip_bf16.h>
#include <stdint.h>
#include <type_traits>
#include <utility>

#define LAS __attribute__((address_space(3)))
typedef unsigned short u16;
typedef __bf16 bf16x4 __attribute__((ext_vector_type(4)));
typedef __bf16 bf16x8 __attribute__((ext_vector_type(8)));
typedef float f32x4 __attribute__((ext_vector_type(4)));
typedef float f32x2 __attribute__((ext_vector_type(2)));
typedef unsigned u32x4 __attribute__((ext_vector_type(4)));
typedef unsigned u32x2 __attribute__((ext_vector_type(2)));
typedef __bf16 bf16x2 __attribute__((ext_vector_type(2)));

constexpr int S_ = 2048, D_ = 1024, F_ = 3584;

__device__ __forceinline__ unsigned pack_bf16(float lo, float hi) {
  bf16x2 v; v[0] = (__bf16)lo; v[1] = (__bf16)hi;
  return __builtin_bit_cast(unsigned, v);
}
__device__ __forceinline__ float bf_lo(unsigned u) { return __builtin_bit_cast(float, u << 16); }
__device__ __forceinline__ float bf_hi(unsigned u) { return __builtin_bit_cast(float, u & 0xffff0000u); }

#define WAIT_V(n) asm volatile("s_waitcnt vmcnt(%0)" ::"i"(n) : "memory")
#define WAIT_L0() asm volatile("s_waitcnt lgkmcnt(0)" ::: "memory")
#define BAR() asm volatile("s_barrier" ::: "memory")
#define SB() __builtin_amdgcn_sched_barrier(0)
template <class T> __device__ __forceinline__ void PIN(T& x) { asm volatile("" : "+v"(x)); }

#define TSDECL
#define TSARG
#define TSPASS
#define TSTAMP(s)
#define TSOUT(k, n)

template <int... I, class Fn>
__device__ __forceinline__ void sfor_impl(std::integer_sequence<int, I...>, Fn&& f) { (f(std::integral_constant<int, I>{}), ...); }
template <int N, class Fn>
__device__ __forceinline__ void sfor(Fn&& f) { sfor_impl(std::make_integer_sequence<int, N>{}, f); }

__device__ __forceinline__ u32x4 mk_srd(const void* p, unsigned bytes) {
  const unsigned long long a = (unsigned long long)p;
  u32x4 r;
  r[0] = __builtin_amdgcn_readfirstlane((unsigned)a);
  r[1] = __builtin_amdgcn_readfirstlane((unsigned)(a >> 32));
  r[2] = bytes;
  r[3] = 0x00020000u;
  return r;
}
__device__ __forceinline__ void glds16(const u32x4& srd, unsigned lds_dst, unsigned voff, unsigned soff) {
  asm volatile("s_mov_b32 m0, %0\n\ts_nop 0\n\tbuffer_load_dwordx4 %1, %2, %3 offen lds" ::"s"(lds_dst), "v"(voff), "s"(srd), "s"(soff) : "memory");
}
template <int IMM, class T>
__device__ __forceinline__ void gld16(T& dst, const u32x4& srd, unsigned voff, unsigned soff) {
  asm volatile("buffer_load_dwordx4 %0, %1, %2, %3 offen offset:%4" : "=v"(dst) : "v"(voff), "s"(srd), "s"(soff), "i"(IMM) : "memory");
}
template <int IMM, class T>
__device__ __forceinline__ void gld8(T& dst, const u32x4& srd, unsigned voff, unsigned soff) {
  asm volatile("buffer_load_dwordx2 %0, %1, %2, %3 offen offset:%4" : "=v"(dst) : "v"(voff), "s"(srd), "s"(soff), "i"(IMM) : "memory");
}
__device__ __forceinline__ void gld4(float& dst, const u32x4& srd, unsigned voff, unsigned soff) {
  asm volatile("buffer_load_dword %0, %1, %2, %3 offen" : "=v"(dst) : "v"(voff), "s"(srd), "s"(soff) : "memory");
}
__device__ __forceinline__ void glds4(const u32x4& srd, unsigned lds_dst, unsigned voff, unsigned soff) {
  asm volatile("s_mov_b32 m0, %0\n\ts_nop 0\n\tbuffer_load_dword %1, %2, %3 offen lds" ::"s"(lds_dst), "v"(voff), "s"(srd), "s"(soff) : "memory");
}
__device__ __forceinline__ bf16x8 tr_frag(LAS char* p0, LAS char* p1) {
  const bf16x4 lo = __builtin_amdgcn_ds_read_tr16_b64_v4bf16((LAS bf16x4*)p0);
  const bf16x4 hi = __builtin_amdgcn_ds_read_tr16_b64_v4bf16((LAS bf16x4*)p1);
  return __builtin_shufflevector(lo, hi, 0, 1, 2, 3, 4, 5, 6, 7);
}
template <bool WT> __device__ __forceinline__ void gst16(void* p, const u32x4& v) {
  if (WT) asm volatile("global_store_dwordx4 %0, %1, off sc1\n\ts_nop 1" ::"v"(p), "v"(v) : "memory");
  else *(u32x4*)p = v;
}
template <bool WT> __device__ __forceinline__ void gst8(void* p, const u32x2& v) {
  if (WT) asm volatile("global_store_dwordx2 %0, %1, off sc1\n\ts_nop 1" ::"v"(p), "v"(v) : "memory");
  else *(u32x2*)p = v;
}
#define MFMA(a, b, c) __builtin_amdgcn_mfma_f32_16x16x32_bf16(a, b, c, 0, 0, 0)
__device__ __forceinline__ void mfma_acc(f32x4& c, const bf16x8& a, const bf16x8& b) {
  asm volatile("v_mfma_f32_16x16x32_bf16 %0, %1, %2, %0" : "+a"(c) : "v"(a), "v"(b));
}
__device__ __forceinline__ void PINA(f32x4& c) { asm volatile("" : "+a"(c)); }

template <int MF, int KS, int BI, int ST, int AD, int XR, int PRO, int XE>
struct Sched {
  static constexpr int NS = 4 * AD, BP = (BI + KS - 1) / KS, MAXX = 64;
  int total[NS + 1] = {};
  int posA[NS + AD + 1] = {}, posB[NS + ST + 2] = {};
  int nx[AD] = {};
  int xkind[AD][MAXX] = {}, xidx[AD][MAXX] = {}, xpc[AD][MAXX] = {};
  int npro = 0, pkind[MAXX] = {}, pidx[MAXX] = {}, ppc[MAXX] = {};
  constexpr Sched() {
    int n = 0;
    int qk[512] = {}, qi[512] = {}, qp[512] = {}, nq = 0, hq = 0;
    for (int p = 0; p < BI; ++p) { qk[nq] = 1; qi[nq] = 0; qp[nq] = p; ++nq; }
    for (int a = 0; a < AD || (a + 1) / KS < ST; ++a) {
      if ((a + 1) % KS == 0 && (a + 1) / KS < ST) for (int p = 0; p < BI; ++p) { qk[nq] = 1; qi[nq] = (a + 1) / KS; qp[nq] = p; ++nq; }
      if (a < AD) for (int m = 0; m < MF; ++m) { qk[nq] = 0; qi[nq] = a; qp[nq] = m; ++nq; }
    }
    while (hq < nq && hq < PRO) {
      pkind[hq] = qk[hq]; pidx[hq] = qi[hq]; ppc[hq] = qp[hq];
      ++n; if (qk[hq]) posB[qi[hq]] = n; else posA[qi[hq]] = n;
      ++hq;
    }
    npro = hq;
    for (int j = 0; j < NS; ++j) {
      total[j] = n;
      if (j < AD) {
        int c = 0;
        while (hq < nq && (c < XR || j == AD - 1)) {
          xkind[j][c] = qk[hq]; xidx[j][c] = qi[hq]; xpc[j][c] = qp[hq];
          ++n; if (qk[hq]) posB[qi[hq]] = n; else posA[qi[hq]] = n;
          ++hq; ++c;
        }
        nx[j] = c;
      }
      if (XE && j >= AD && j % XE == 0) ++n;
      if (j >= 1) { n += MF; posA[j - 1 + AD] = n; }
      if (j + 1 >= KS) {
        const int g = (j + 1) % KS, T = (j + 1) / KS - 1 + ST;
        for (int p = g * BP; p < (g + 1) * BP && p < BI; ++p) { ++n; posB[T] = n; }
      }
    }
    total[NS] = n;
  }
  constexpr int clampv(int v) const { return v > 63 ? 63 : v; }
  constexpr int waitA(int j) const { return clampv(total[j] - posA[j]); }
  constexpr int waitB(int j) const { return clampv(total[j] - posB[j / KS + 1]); }
  constexpr int min3(int a, int b, int c) const { return a < b ? (a < c ? a : c) : (b < c ? b : c); }
  constexpr int loopA(int r) const { return min3(waitA(AD + r), waitA(2 * AD + r), waitA(3 * AD + r)); }
  constexpr int loopB(int r) const { return min3(waitB(AD + r), waitB(2 * AD + r), waitB(3 * AD + r)); }
  constexpr bool ok() const {
    for (int j = 0; j < NS; ++j) {
      if (posA[j] <= 0 || posA[j] > total[j]) return false;
      if (j % KS == KS - 1 && (posB[j / KS + 1] <= 0 || posB[j / KS + 1] > total[j])) return false;
    }
    for (int j = 0; j < AD; ++j) if (nx[j] > MAXX) return false;
    if (posB[0] > total[0] || PRO > MAXX || 3 * AD / KS + 1 < ST) return false;
    return true;
  }
};

template <int MODE_, int MF_, int NF_, int KS_, int ST_, int AD_, int XR_, int PRO_, int KTOT_, int KLD_, int XE_, int MU_ = 0>
struct Cfg {
  static constexpr int MODE = MODE_, MF = MF_, NF = NF_, KS = KS_, ST = ST_, AD = AD_, XR = XR_, PRO = PRO_, KTOT = KTOT_, KLD = KLD_, XE = XE_, NFX = NF_ + MU_;
  static constexpr int BK = 32 * KS;
  static constexpr int ROWB = (MODE == 3) ? 224 : NF * 32;
  static constexpr int SUB = (MODE == 3) ? BK * ROWB : 0;
  static constexpr int STAGE = BK * NF * 32;
  static constexpr int BI = STAGE / 4096;
  static constexpr int LDS_BYTES = ST * STAGE;
  static_assert((XE == 0 || AD % XE == 0) && STAGE % 4096 == 0 && AD % KS == 0 && AD % 2 == 0 && (KTOT / 32) % AD == 0, "");
};

template <class C> struct SchedOf {
  using SC = Sched<C::MF, C::KS, C::BI, C::ST, C::AD, C::XR, C::PRO, C::XE>;
  static constexpr SC S{};
};
template <class C> __device__ __forceinline__ int b_swz(int row) {
  if (C::MODE == 3) return 0;
  if (C::MODE == 1) return ((row >> 1) & 1) | (((row >> 3) & 1) << 1);
  if (C::ROWB % 256 == 0) return row & 7;
  return (row >> 1) & 3;
}

template <class C, class Mid>
__device__ __forceinline__ void gemm_main(f32x4 (&acc)[C::MF][C::NFX], LAS char* lds, const u16* A, const u16* B0, const u16* B1, unsigned b_bytes,
                                          int ldb, const u32x4& swarm, Mid&& mid TSARG) {
  constexpr int MF = C::MF, NF = C::NFX, KS = C::KS, ST = C::ST, AD = C::AD, BI = C::BI, ROWB = C::ROWB, NMM = MF * NF;
  using SO = SchedOf<C>;
  static_assert(SO::S.ok(), "schedule");
  constexpr int BP = SO::SC::BP;
  TSTAMP(0);
  const int lane = threadIdx.x & 63;
  const int wave = __builtin_amdgcn_readfirstlane(threadIdx.x >> 6);
  const unsigned lds0 = (unsigned)(size_t)lds;
  const u32x4 sa = mk_srd(A + (size_t)wave * MF * 16 * C::KLD, (unsigned)((MF - 1) * 16 * C::KLD + 16 * C::KTOT) * 2);
  const u32x4 sb = mk_srd((C::MODE == 3 && wave >= 2) ? B1 : B0, b_bytes);
  unsigned a_off[MF];
#pragma unroll
  for (int m = 0; m < MF; ++m) a_off[m] = lane * 16 + m * (C::KLD * 32);
  unsigned b_src[BI];
#pragma unroll
  for (int i = 0; i < BI; ++i) {
    if (C::MODE == 3) {
      const int ch = ((wave & 1) * BI + i) * 64 + lane, row = ch / 14, c = ch % 14;
      b_src[i] = (unsigned)(row * ldb * 2 + c * 16);
    } else {
      constexpr int CPR = ROWB / 16;
      const int ch = (wave * BI + i) * 64 + lane, row = ch / CPR, cs = ch % CPR, c = cs ^ (b_swz<C>(row) << 1);
      const int col = (C::MODE == 1) ? (c >> 3) * 1024 + (c & 7) * 8 : c * 8;
      b_src[i] = (unsigned)(row * ldb * 2 + col * 2);
    }
  }
  const unsigned b_dst = lds0 + wave * BI * 1024;
  const unsigned b_tile = (unsigned)(C::BK * ldb * 2);

  const int G = lane >> 4, kr = (lane & 15) >> 2, p = lane & 3;
  const int r0 = (C::MODE == 1 ? 8 : 4) * G + kr;
  constexpr int RD2 = (C::MODE == 1 ? 4 : 16) * ROWB;
  constexpr int NV = (C::MODE == 3) ? 1 : (ROWB % 256 == 0 ? 8 : 4);
  int b_rd[NV];
#pragma unroll
  for (int v = 0; v < NV; ++v) b_rd[v] = r0 * ROWB + ((v ^ b_swz<C>(r0)) << 5) + p * 8;
  LAS char* xfr = lds + C::LDS_BYTES + (lane & 1) * 64 + G * 16;
  auto half = [&](LAS char* base, int n, int s, int hf, int step) -> bf16x4 {
    if (n == C::NF) return *(const LAS bf16x4*)(xfr + step * 128 + hf * 8);
    int off;
    if (C::MODE == 3) off = b_rd[0] + (n & 1) * C::SUB + (n >> 1) * 32;
    else off = b_rd[n % NV] + (n / NV) * (NV * 32);
    off += s * 32 * ROWB + hf * RD2;
    return __builtin_amdgcn_ds_read_tr16_b64_v4bf16((LAS bf16x4*)(base + off));
  };

  bf16x8 ar[AD][MF];
  bf16x4 bf[2][NF][2];
  auto issueA = [&](auto RC, auto MC, unsigned step) { gld16<0>(ar[decltype(RC)::value][decltype(MC)::value], sa, a_off[decltype(MC)::value], step < C::KTOT / 32 ? step * 1024 : 0x7ff00000u); };
  if constexpr (C::NFX > C::NF) glds16(swarm, lds0 + C::LDS_BYTES + wave * 1024, lane * 16, wave * 1024);
  sfor<SO::S.npro>([&](auto LC) {
    constexpr int l = decltype(LC)::value, kind = SO::S.pkind[l], idx = SO::S.pidx[l], pc = SO::S.ppc[l];
    if constexpr (kind) glds16(sb, b_dst + idx * C::STAGE + pc * 1024, b_src[pc], idx * b_tile);
    else issueA(std::integral_constant<int, idx>{}, std::integral_constant<int, pc>{}, idx);
  });
#pragma unroll
  for (int m = 0; m < MF; ++m)
#pragma unroll
    for (int n = 0; n < NF; ++n) { acc[m][n] = f32x4{0.f, 0.f, 0.f, 0.f}; PINA(acc[m][n]); }
  TSTAMP(1);
  { constexpr int w0 = SO::S.total[0] - SO::S.posB[0]; WAIT_V(w0); }
  BAR();
  TSTAMP(2);
#pragma unroll
  for (int n = 0; n < NF; ++n) { bf[0][n][0] = half(lds, n, 0, 0, 0); bf[0][n][1] = half(lds, n, 0, 1, 0); }
  SB();

  int slot = 0, fslot = 0;
  auto step = [&](auto JC, auto FC, int j0) {
    constexpr int R = decltype(JC)::value;
    constexpr bool FIRST = decltype(FC)::value;
    constexpr int s = R % KS, cur = R & 1, nxt = cur ^ 1;
    constexpr int WA = FIRST ? SO::S.waitA(R) : SO::S.loopA(R), WB = (R % KS != KS - 1) ? 63 : (FIRST ? SO::S.waitB(R) : SO::S.loopB(R));
    WAIT_V(WA);
    sfor<MF>([&](auto m) { PIN(ar[R][decltype(m)::value]); });
    if constexpr (s == KS - 1) {
      WAIT_L0();
      if constexpr (WB < WA) WAIT_V(WB);
      BAR();
      fslot = slot;
      slot = (slot + 1 == ST) ? 0 : slot + 1;
    }
    LAS char* rbase = lds + slot * C::STAGE;
    constexpr int ns = (s + 1) % KS;
    constexpr bool HASB = !(FIRST && R + 1 < KS);
    constexpr int g = (R + 1) % KS;
    constexpr int NB = HASB ? ((g + 1) * BP <= BI ? BP : (BI - g * BP > 0 ? BI - g * BP : 0)) : 0;
    constexpr int NX = FIRST ? SO::S.nx[R] : (C::XE && R % (C::XE ? C::XE : 1) == 0 ? 1 : 0);
    constexpr int NA = (FIRST && R == 0) ? 0 : MF, RP = (R + AD - 1) % AD;
    constexpr int NL = NX + NA + NB;
    const unsigned gdst = b_dst + fslot * C::STAGE;
    const unsigned gsoff = (unsigned)((j0 + R + 1) / KS - 1 + ST) * b_tile;
    SB();
    sfor<NMM>([&](auto IC) {
      constexpr int i = decltype(IC)::value, n = i / MF, m = i % MF;
      const bf16x8 bb = __builtin_shufflevector(bf[cur][n][0], bf[cur][n][1], 0, 1, 2, 3, 4, 5, 6, 7);
      if constexpr (C::MODE == 1 && n >= 4) mfma_acc(acc[m][n], ar[R][m], bb);
      else mfma_acc(acc[m][n], bb, ar[R][m]);
      SB();
      constexpr int SPAN = (NMM * 7) / 8 >= 2 * NF ? (NMM * 7) / 8 : NMM;
      sfor<2 * NF>([&](auto NC) {
        constexpr int nn = decltype(NC)::value;
        if constexpr ((nn * SPAN) / (2 * NF) == i) bf[nxt][nn >> 1][nn & 1] = half(rbase, nn >> 1, ns, nn & 1, j0 + R + 1);
      });
      sfor<NL>([&](auto LC) {
        constexpr int l = decltype(LC)::value;
        if constexpr (((2 * l + 1) * NMM) / (2 * NL) == i) {
          if constexpr (l < NX && !FIRST) {
            glds4(swarm, lds0 + C::LDS_BYTES + wave * 256, lane * 64, (unsigned)(j0 + R - AD) * (4096 / (C::XE ? C::XE : 1)));
          } else if constexpr (l < NX) {
            constexpr int kind = SO::S.xkind[R][l], idx = SO::S.xidx[R][l], pc = SO::S.xpc[R][l];
            if constexpr (kind) glds16(sb, b_dst + idx * C::STAGE + pc * 1024, b_src[pc], idx * b_tile);
            else issueA(std::integral_constant<int, idx>{}, std::integral_constant<int, pc>{}, idx);
          } else if constexpr (l < NX + NA) {
            issueA(std::integral_constant<int, RP>{}, std::integral_constant<int, l - NX>{}, (unsigned)(j0 + R - 1 + AD));
          } else {
            constexpr int pc = g * BP + (l - NX - NA);
            glds16(sb, gdst + pc * 1024, b_src[pc], gsoff);
          }
        }
      });
      SB();
    });
  };
  sfor<AD>([&](auto r) { step(r, std::true_type{}, 0); });
  mid();
  SB();
#pragma unroll 1
  for (int j0 = AD; j0 < C::KTOT / 32; j0 += AD) sfor<AD>([&](auto r) { step(r, std::false_type{}, j0); });
  asm volatile("s_nop 15\n\ts_nop 15" ::: "memory");
#pragma unroll
  for (int m = 0; m < MF; ++m)
#pragma unroll
    for (int n = 0; n < NF; ++n) PINA(acc[m][n]);
  TSTAMP(3);
  WAIT_V(0);
  sfor<AD>([&](auto r) { sfor<MF>([&](auto m) { PIN(ar[decltype(r)::value][decltype(m)::value]); }); });
  WAIT_L0();
  TSTAMP(5);
}
#define C1_ST 4
#define C1_AD 4
#define C1_XR 6
#define C2_ST 4
#define C2_XR 3
#define C3_ST 4
#define C3_XR 10
#define C4_ST 4
#define C4_XR 3
#define C1_PRO 16
#define C2_PRO 16
#define C3_PRO 18
#define C4_PRO 16
#define MAP4 0
#define WTALL 1
constexpr bool WT0 = WTALL, WT1 = WTALL, WTO = WTALL, WT2 = WTALL, WT3 = true, WT4 = WTALL;
#define PFMODE 0
#define C1_XE (PFMODE == 1 ? 0 : 4)
#define C3_XE 4

__global__ __launch_bounds__(256) void ln1_kernel(const u16* __restrict__ x, u16* __restrict__ y, const u16* __restrict__ wqkv,
                                                  const u16* __restrict__ wo, float* __restrict__ sumx, u16* __restrict__ rsw) {
  __shared__ float red[2][4][8];
  const int t = threadIdx.x, wave = t >> 6, r = t & 7, c0 = t >> 3, row = blockIdx.x * 8 + r;
  u32x4 v[4], pf[6];
  const u32x4 sx = mk_srd(x + (size_t)blockIdx.x * 8 * D_, 8 * D_ * 2);
  sfor<4>([&](auto i) { gld16<decltype(i)::value * 512>(v[decltype(i)::value], sx, r * D_ * 2 + c0 * 16, 0); });
  const u32x4 s1 = mk_srd(wqkv + (size_t)blockIdx.x * 12288, 24576);
  sfor<6>([&](auto i) { gld16<0>(pf[decltype(i)::value], s1, t * 16, decltype(i)::value * 4096); });
  u32x4 wr[2];
  const u32x4 s2 = mk_srd(wo + (size_t)blockIdx.x * 4096, 8192);
  gld16<0>(wr[0], s2, t * 32, 0);
  gld16<16>(wr[1], s2, t * 32, 0);
  WAIT_V(8);
  sfor<4>([&](auto i) { PIN(v[decltype(i)::value]); });
  float s = 0.f;
#pragma unroll
  for (int i = 0; i < 4; ++i)
#pragma unroll
    for (int j = 0; j < 4; ++j) s += bf_lo(v[i][j]) + bf_hi(v[i][j]);
  s += __shfl_xor(s, 8); s += __shfl_xor(s, 16); s += __shfl_xor(s, 32);
  if ((t & 63) < 8) red[0][wave][r] = s;
  __syncthreads();
  const float sx4 = red[0][0][r] + red[0][1][r] + red[0][2][r] + red[0][3][r];
  const float m = sx4 * (1.f / D_);
  if (t < 8) sumx[row] = sx4;
  float q = 0.f;
#pragma unroll
  for (int i = 0; i < 4; ++i)
#pragma unroll
    for (int j = 0; j < 4; ++j) { const float a = bf_lo(v[i][j]) - m, b = bf_hi(v[i][j]) - m; q += a * a + b * b; }
  q += __shfl_xor(q, 8); q += __shfl_xor(q, 16); q += __shfl_xor(q, 32);
  if ((t & 63) < 8) red[1][wave][r] = q;
  __syncthreads();
  const float var = (red[1][0][r] + red[1][1][r] + red[1][2][r] + red[1][3][r]) * (1.f / D_);
  const float rs = 1.f / sqrtf(var + 1e-5f);
#pragma unroll
  for (int i = 0; i < 4; ++i) {
    u32x4 o;
#pragma unroll
    for (int j = 0; j < 4; ++j) o[j] = pack_bf16((bf_lo(v[i][j]) - m) * rs, (bf_hi(v[i][j]) - m) * rs);
    const int c = c0 + 32 * i;
    gst16<WT0>(y + ((size_t)((row >> 4) * 32 + (c >> 2)) * 64 + (c & 3) * 16 + (row & 15)) * 8, o);
  }
  WAIT_V(0);
  sfor<6>([&](auto i) { PIN(pf[decltype(i)::value]); });
  PIN(wr[0]); PIN(wr[1]);
  float ws = 0.f;
#pragma unroll
  for (int j = 0; j < 4; ++j) ws += (bf_lo(wr[0][j]) + bf_hi(wr[0][j])) + (bf_lo(wr[1][j]) + bf_hi(wr[1][j]));
#pragma unroll
  for (int d = 1; d < 64; d <<= 1) ws += __shfl_xor(ws, d);
  if ((t & 63) == 0) {
    const int k = blockIdx.x * 4 + wave, kk = k & 31;
    const unsigned hi = pack_bf16(ws, 0.f), lo = pack_bf16(ws - bf_lo(hi), 0.f);
    u16* dst = rsw + (k >> 5) * 64 + ((kk & 15) >> 2) * 8 + (kk & 3) + (kk >> 4) * 4;
    dst[0] = (u16)hi;
    dst[32] = (u16)lo;
  }
}

__device__ __forceinline__ float phi(float t) {
  const float e = __builtin_amdgcn_fmed3f(__builtin_amdgcn_exp2f(t * 1.4426950408889634f), 0.f, 1.f);
  return (fmaxf(t, 0.f) + e) + 1e-6f;
}
__device__ __forceinline__ bf16x8 pack8(const f32x4& a, const f32x4& b) {
  u32x4 o = {pack_bf16(a[0], a[1]), pack_bf16(a[2], a[3]), pack_bf16(b[0], b[1]), pack_bf16(b[2], b[3])};
  return __builtin_bit_cast(bf16x8, o);
}
__device__ __forceinline__ f32x4 phi4(const f32x4& a) { return f32x4{phi(a[0]), phi(a[1]), phi(a[2]), phi(a[3])}; }

constexpr int KVP = 1024 + 32;
using C1 = Cfg<1, 2, 12, 2, C1_ST, C1_AD, C1_XR, C1_PRO, 1024, 1024, C1_XE>;
__global__ __launch_bounds__(256, 1) void gemm1_kernel(const u16* __restrict__ xn, const u16* __restrict__ wqkv, u16* __restrict__ pq,
                                                       float* __restrict__ kvp, const u16* __restrict__ wg,
                                                       const u16* __restrict__ wu) {
  extern __shared__ __attribute__((aligned(16))) char smem[];
  LAS char* lds = (LAS char*)smem;
  const int b = blockIdx.x, xcd = b & 7, idx = b >> 3;
  const int tm = (xcd >> 2) * 8 + (idx & 7), tn = (xcd & 3) * 4 + (idx >> 3);
  f32x4 acc[2][12];
  TSDECL;
  const int lane = threadIdx.x & 63, wave = __builtin_amdgcn_readfirstlane(threadIdx.x >> 6), gw = b * 4 + wave;
  const u32x4 sw = mk_srd(gw < 512 ? wg + gw * 7168 : wu + (gw - 512) * 7168, 14336);
  gemm_main<C1>(acc, lds, xn + (size_t)tm * 128 * D_, wqkv + tn * 64, wqkv, (unsigned)(D_ * 3 * D_ - tn * 64) * 2, 3 * D_, sw, [] {} TSPASS);
#pragma unroll
  for (int m = 0; m < 2; ++m)
#pragma unroll
    for (int hh = 0; hh < 2; ++hh)
      gst16<WT1>(pq + ((size_t)((tm * 8 + wave * 2 + m) * 32 + tn * 2 + hh) * 64 + lane) * 8, __builtin_bit_cast(u32x4, pack8(phi4(acc[m][2 * hh]), phi4(acc[m][2 * hh + 1]))));
  BAR();
  LAS f32x4* red = (LAS f32x4*)lds;
  const u32x4 one4 = {0x3f803f80u, 0x3f803f80u, 0x3f803f80u, 0x3f803f80u};
  const f32x4 z4 = {0.f, 0.f, 0.f, 0.f};
#pragma unroll
  for (int hh = 0; hh < 2; ++hh) {
    const bf16x8 v0 = pack8(acc[0][8 + 2 * hh], acc[1][8 + 2 * hh]), v1 = pack8(acc[0][9 + 2 * hh], acc[1][9 + 2 * hh]);
#pragma unroll
    for (int i = 0; i < 2; ++i) {
      const bf16x8 pk = pack8(phi4(acc[0][4 + 2 * hh + i]), phi4(acc[1][4 + 2 * hh + i]));
      red[((wave * 2 + hh) * 6 + 2 * i) * 64 + lane] = MFMA(pk, v0, z4);
      red[((wave * 2 + hh) * 6 + 2 * i + 1) * 64 + lane] = MFMA(pk, v1, z4);
      red[((wave * 2 + hh) * 6 + 4 + i) * 64 + lane] = MFMA(pk, __builtin_bit_cast(bf16x8, one4), z4);
    }
  }
  WAIT_L0();
  BAR();
#pragma unroll
  for (int e = 0; e < 3; ++e) {
    const int blk = e * 4 + wave, hh = blk / 6, bb = blk % 6;
    const f32x4 s = (red[blk * 64 + lane] + red[(12 + blk) * 64 + lane]) + (red[(24 + blk) * 64 + lane] + red[(36 + blk) * 64 + lane]);
    float* dst = kvp + ((size_t)(tn * 2 + hh) * 16 + tm) * KVP;
    if (bb < 4) gst16<WT1>(dst + bb * 256 + lane * 4, __builtin_bit_cast(u32x4, s));
    else if ((lane & 15) == 0) gst16<WT1>(dst + 1024 + (bb - 4) * 16 + (lane >> 4) * 4, __builtin_bit_cast(u32x4, s));
  }
  WAIT_V(0);
  TSTAMP(4);
  TSOUT(0, 6);
}

__global__ __launch_bounds__(256) void o_kernel(const u16* __restrict__ pq, const float* __restrict__ kvp, u16* __restrict__ o,
                                                const u16* __restrict__ wu) {
  __shared__ __attribute__((aligned(16))) float sh[1024];
  const int t = threadIdx.x, lane = t & 63, wave = __builtin_amdgcn_readfirstlane(t >> 6);
  const int head = blockIdx.x & 31, rg = blockIdx.x >> 5;
  const u32x4 sk = mk_srd(kvp + (size_t)head * 16 * KVP, 16 * KVP * 4), sq = mk_srd(pq + ((size_t)(rg * 16 + wave * 4) * 32 + head) * 512, 100 * 1024);
  const u32x4 sw = mk_srd(wu + (blockIdx.x * 4 + wave) * 3584, PFMODE ? 7168 : 0);
  f32x4 pv[16], zp;
  bf16x8 qf[4];
  float pf[2];
  sfor<16>([&](auto i) { gld16<0>(pv[decltype(i)::value], sk, t * 16, decltype(i)::value * KVP * 4); });
  gld16<0>(zp, sk, t < 128 ? (t >> 3) * KVP * 4 + 4096 + (t & 7) * 16 : 0x7ffffff0u, 0);
  sfor<4>([&](auto m) { gld16<0>(qf[decltype(m)::value], sq, lane * 16, decltype(m)::value * 32768); });
  gld4(pf[0], sw, lane * 64, 0);
  gld4(pf[1], sw, lane * 64, 4096);
  WAIT_V(2);
  sfor<16>([&](auto i) { PIN(pv[decltype(i)::value]); });
  sfor<4>([&](auto m) { PIN(qf[decltype(m)::value]); });
  PIN(zp);
#pragma unroll
  for (int st = 8; st > 0; st >>= 1)
#pragma unroll
    for (int i = 0; i < st; ++i) pv[i] += pv[i + st];
  u32x2 pk2 = {pack_bf16(pv[0][0], pv[0][1]), pack_bf16(pv[0][2], pv[0][3])};
  *(u32x2*)((char*)sh + ((wave & 1) * 64 + lane) * 16 + (wave >> 1) * 8) = pk2;
  if (t < 128) *(f32x4*)(sh + 512 + t * 4) = zp;
  __syncthreads();
  const bf16x8 kv0 = *(const bf16x8*)((char*)sh + lane * 16), kv1 = *(const bf16x8*)((char*)sh + (64 + lane) * 16);
  f32x4 za = {0.f, 0.f, 0.f, 0.f}, zb = za;
#pragma unroll
  for (int i = 0; i < 16; ++i) {
    za += *(const f32x4*)(sh + 512 + i * 32 + (lane >> 4) * 4);
    zb += *(const f32x4*)(sh + 512 + i * 32 + 16 + (lane >> 4) * 4);
  }
  const bf16x8 zf = pack8(za, zb);
  const f32x4 z4 = {0.f, 0.f, 0.f, 0.f};
#pragma unroll
  for (int m = 0; m < 4; ++m) {
    const f32x4 n0 = MFMA(kv0, qf[m], z4), n1 = MFMA(kv1, qf[m], z4), dn = MFMA(zf, qf[m], z4);
    const float inv = 1.f / (dn[0] + 1e-6f);
    gst16<WTO>(o + ((size_t)((rg * 16 + wave * 4 + m) * 32 + head) * 64 + lane) * 8, __builtin_bit_cast(u32x4, pack8(n0 * inv, n1 * inv)));
  }
  WAIT_V(0);
  PIN(pf[0]); PIN(pf[1]);
}

using C2 = Cfg<2, 2, 4, 4, C2_ST, 8, C2_XR, C2_PRO, 1024, 1024, 0, 1>;
constexpr int LDS2 = C2::LDS_BYTES + 4096 + 256;
__global__ __launch_bounds__(256, 1) void gemm2_kernel(const u16* __restrict__ o, const u16* __restrict__ wo, const u16* __restrict__ x,
                                                       u16* __restrict__ h, float* __restrict__ stats, const float* __restrict__ sumx,
                                                       const u16* __restrict__ rsw, float* __restrict__ mub) {
  extern __shared__ __attribute__((aligned(16))) char smem[];
  LAS char* lds = (LAS char*)smem;
  const int b = blockIdx.x, xcd = b & 7, idx = b >> 3;
  const int tm = (xcd >> 1) * 4 + (idx & 3), tn = (xcd & 1) * 8 + (idx >> 2);
  const int lane = threadIdx.x & 63, wave = __builtin_amdgcn_readfirstlane(threadIdx.x >> 6);
  f32x4 acc[2][5];
  u32x2 res[2][4];
  float sxr[2];
  const u32x4 sx = mk_srd(x + (size_t)(tm * 128 + wave * 32) * D_ + tn * 64, 32 * D_ * 2);
  const u32x4 ssx = mk_srd(sumx + tm * 128 + wave * 32, 128), sr = mk_srd(rsw, 4096);
  const unsigned xo = (unsigned)((lane & 15) * D_ * 2 + (lane >> 4) * 8);
  TSDECL;
  gemm_main<C2>(acc, lds, o + (size_t)tm * 128 * D_, wo + tn * 64, wo, (unsigned)(D_ * D_ - tn * 64) * 2, D_, sr, [&] {
    sfor<2>([&](auto m) {
      sfor<4>([&](auto n) { gld8<decltype(n)::value * 32>(res[decltype(m)::value][decltype(n)::value], sx, xo, decltype(m)::value * 16 * D_ * 2); });
      gld4(sxr[decltype(m)::value], ssx, (lane & 15) * 4, decltype(m)::value * 64);
    });
  } TSPASS);
#pragma unroll
  for (int m = 0; m < 2; ++m) {
    unsigned w[8];
    PIN(sxr[m]);
    const float mu = (sxr[m] + (acc[m][4][0] + acc[m][4][1])) * (1.f / D_);
#pragma unroll
    for (int n = 0; n < 4; ++n) {
      PIN(res[m][n]);
      w[2 * n] = pack_bf16(bf_lo(res[m][n][0]) + acc[m][n][0] - mu, bf_hi(res[m][n][0]) + acc[m][n][1] - mu);
      w[2 * n + 1] = pack_bf16(bf_lo(res[m][n][1]) + acc[m][n][2] - mu, bf_hi(res[m][n][1]) + acc[m][n][3] - mu);
    }
    const int mb = tm * 8 + wave * 2 + m;
    gst16<WT2>(h + ((size_t)(mb * 32 + tn * 2) * 64 + lane) * 8, u32x4{w[0], w[1], w[2], w[3]});
    gst16<WT2>(h + ((size_t)(mb * 32 + tn * 2 + 1) * 64 + lane) * 8, u32x4{w[4], w[5], w[6], w[7]});
    float q = 0.f;
#pragma unroll
    for (int i = 0; i < 8; ++i) { const float a = bf_lo(w[i]), c = bf_hi(w[i]); q += a * a + c * c; }
    q += __shfl_xor(q, 16); q += __shfl_xor(q, 32);
    if (lane < 16) {
      stats[(size_t)(mb * 16 + lane) * 16 + tn] = q;
      if (tn == 0) mub[mb * 16 + lane] = mu;
    }
  }
  TSTAMP(4);
  TSOUT(1, 6);
}

using C3 = Cfg<3, 4, 14, 2, C3_ST, 4, C3_XR, C3_PRO, 1024, 1024, C3_XE>;
constexpr int LDS3 = C3::LDS_BYTES + 1024;
template <int PAR>
__device__ __forceinline__ void geglu_store(f32x4 (&acc)[4][14], const float (&var)[4], u16* g, int mb0, int tn, int lane) {
#pragma unroll
  for (int m = 0; m < 4; ++m) {
    const float ir2 = var[m], r = __builtin_amdgcn_rsqf(ir2), k1 = -2.f * 0.7978845608028654f * 1.4426950408889634f * r, k2 = k1 * 0.044715f * r * r;
    char* dst = (char*)g + ((size_t)(mb0 + m) * (F_ / 32) + ((tn * 7) >> 1)) * 1024;
    unsigned w[14];
#pragma unroll
    for (int n = 0; n < 7; ++n) {
      const f32x4 a = acc[m][2 * n], u = acc[m][2 * n + 1];
      const f32x4 arg = a * (a * a * k2 + k1);
      f32x4 d;
#pragma unroll
      for (int i = 0; i < 4; ++i) d[i] = __builtin_amdgcn_exp2f(arg[i]);
      d = d * ir2 + ir2;
#pragma unroll
      for (int i = 0; i < 4; ++i) d[i] = __builtin_amdgcn_rcpf(d[i]);
      const f32x4 o = a * u * d;
      w[2 * n] = pack_bf16(o[0], o[1]);
      w[2 * n + 1] = pack_bf16(o[2], o[3]);
    }
    if (PAR == 0) {
#pragma unroll
      for (int i = 0; i < 3; ++i) gst16<WT3>(dst + i * 1024 + lane * 16, u32x4{w[4 * i], w[4 * i + 1], w[4 * i + 2], w[4 * i + 3]});
      gst8<WT3>(dst + 3 * 1024 + lane * 16, u32x2{w[12], w[13]});
    } else {
      gst8<WT3>(dst + lane * 16 + 8, u32x2{w[0], w[1]});
#pragma unroll
      for (int i = 0; i < 3; ++i) gst16<WT3>(dst + (i + 1) * 1024 + lane * 16, u32x4{w[4 * i + 2], w[4 * i + 3], w[4 * i + 4], w[4 * i + 5]});
    }
  }
}
__global__ __launch_bounds__(256, 1) void gemm3_kernel(const u16* __restrict__ h, const u16* __restrict__ wg, const u16* __restrict__ wu,
                                                       const float* __restrict__ stats, u16* __restrict__ g,
                                                       const u16* __restrict__ wd) {
  extern __shared__ __attribute__((aligned(16))) char smem[];
  LAS char* lds = (LAS char*)smem;
  const int b = blockIdx.x, xcd = b & 7, idx = b >> 3;
  const int tm = idx & 7, tn = xcd * 4 + (idx >> 3);
  const int lane = threadIdx.x & 63, wave = __builtin_amdgcn_readfirstlane(threadIdx.x >> 6);
  f32x4 acc[4][14];
  f32x4 st[4];
  const u32x4 ss = mk_srd(stats + (size_t)(tm * 256 + wave * 64) * 16, 64 * 64);
  const u32x4 sw = mk_srd(wd + (b * 4 + wave) * 3584, 7168);
  TSDECL;
  gemm_main<C3>(acc, lds, h + (size_t)tm * 256 * D_, wg + tn * 112, wu + tn * 112, (unsigned)(D_ * F_ - tn * 112) * 2, F_, sw, [&] {
    sfor<4>([&](auto i) { gld16<decltype(i)::value * 16>(st[decltype(i)::value], ss, lane * 64, 0); });
  } TSPASS);
  float var[4];
  {
    sfor<4>([&](auto i) { PIN(st[decltype(i)::value]); });
    const f32x4 q4 = (st[0] + st[1]) + (st[2] + st[3]);
    const float v = ((q4[0] + q4[1]) + (q4[2] + q4[3])) * (1.f / D_) + 1e-5f;
#pragma unroll
    for (int m = 0; m < 4; ++m) var[m] = __shfl(v, m * 16 + (lane & 15));
  }
  TSTAMP(6);
  if (tn & 1) geglu_store<1>(acc, var, g, tm * 16 + wave * 4, tn, lane);
  else geglu_store<0>(acc, var, g, tm * 16 + wave * 4, tn, lane);
  TSTAMP(4);
  TSOUT(2, 7);
}

using C4 = Cfg<2, 2, 4, 4, C4_ST, 8, C4_XR, C4_PRO, F_, F_, 0>;
__global__ __launch_bounds__(256, 1) void gemm4_kernel(const u16* __restrict__ g, const u16* __restrict__ wd, const u16* __restrict__ h,
                                                       const float* __restrict__ mub, u16* __restrict__ out) {
  extern __shared__ __attribute__((aligned(16))) char smem[];
  LAS char* lds = (LAS char*)smem;
  const int b = blockIdx.x, xcd = b & 7, idx = b >> 3;
  const int tm = (xcd >> 1) * 4 + (idx & 3), tn = (xcd & 1) * 8 + (idx >> 2);
  const int lane = threadIdx.x & 63, wave = __builtin_amdgcn_readfirstlane(threadIdx.x >> 6);
  f32x4 acc[2][4];
  u32x4 res[2][2];
  float mu[2];
  const u32x4 smu = mk_srd(mub + tm * 128 + wave * 32, 128);
  const u32x4 sh = mk_srd(h + ((size_t)(tm * 8 + wave * 2) * 32 + tn * 2) * 512, 34 * 1024);
  TSDECL;
  gemm_main<C4>(acc, lds, g + (size_t)tm * 128 * F_, wd + tn * 64, wd, (unsigned)(F_ * D_ - tn * 64) * 2, D_, sh, [&] {
    sfor<2>([&](auto m) { gld16<0>(res[decltype(m)::value][0], sh, lane * 16, decltype(m)::value * 32768); gld16<1024>(res[decltype(m)::value][1], sh, lane * 16, decltype(m)::value * 32768);
      gld4(mu[decltype(m)::value], smu, (lane & 15) * 4, decltype(m)::value * 64); });
  } TSPASS);
  BAR();
  LAS char* patch = lds + wave * (32 * 144);
#pragma unroll
  for (int m = 0; m < 2; ++m)
#pragma unroll
    for (int n = 0; n < 4; ++n) {
      PIN(res[m][n >> 1]); PIN(mu[m]);
      const unsigned r0 = res[m][n >> 1][(n & 1) * 2], r1 = res[m][n >> 1][(n & 1) * 2 + 1];
      const u32x2 w = {pack_bf16(bf_lo(r0) + mu[m] + acc[m][n][0], bf_hi(r0) + mu[m] + acc[m][n][1]),
                       pack_bf16(bf_lo(r1) + mu[m] + acc[m][n][2], bf_hi(r1) + mu[m] + acc[m][n][3])};
      *(LAS u32x2*)(patch + (m * 16 + (lane & 15)) * 144 + n * 32 + (lane >> 4) * 8) = w;
    }
#pragma unroll
  for (int i = 0; i < 4; ++i) {
    const int row = i * 8 + (lane >> 3);
    const u32x4 w = *(const LAS u32x4*)(patch + row * 144 + (lane & 7) * 16);
    gst16<WT4>(out + (size_t)(tm * 128 + wave * 32 + row) * D_ + tn * 64 + (lane & 7) * 8, w);
  }
  TSTAMP(4);
  TSOUT(3, 6);
}

extern "C" void kernel_launch(void* const* d_in, const int* in_sizes, int n_in, void* d_out, int out_size, void* d_ws, size_t ws_size,
                              hipStream_t stream) {
  const u16* x = (const u16*)d_in[0];
  const u16* wqkv = (const u16*)d_in[1];
  const u16* wo = (const u16*)d_in[2];
  const u16* wg = (const u16*)d_in[4];
  const u16* wu = (const u16*)d_in[5];
  const u16* wd = (const u16*)d_in[6];
  u16* out = (u16*)d_out;
  char* ws = (char*)d_ws;
  const size_t MB = 1 << 20;
  u16* xn = (u16*)(ws);
  u16* pq = (u16*)(ws + 4 * MB);
  u16* o = (u16*)(ws + 8 * MB);
  u16* hbuf = (u16*)(ws + 12 * MB);
  float* kvp = (float*)(ws + 16 * MB);
  float* stats = (float*)(ws + 20 * MB);
  float* sumx = (float*)(ws + 21 * MB);
  float* mub = (float*)(ws + 21 * MB + 65536);
  u16* rsw = (u16*)(ws + 21 * MB + 131072);
  u16* g = (u16*)(ws + 24 * MB);

  (void)hipFuncSetAttribute((const void*)gemm1_kernel, hipFuncAttributeMaxDynamicSharedMemorySize, C1::LDS_BYTES + 1024);
  (void)hipFuncSetAttribute((const void*)gemm2_kernel, hipFuncAttributeMaxDynamicSharedMemorySize, LDS2);
  (void)hipFuncSetAttribute((const void*)gemm3_kernel, hipFuncAttributeMaxDynamicSharedMemorySize, LDS3);
  (void)hipFuncSetAttribute((const void*)gemm4_kernel, hipFuncAttributeMaxDynamicSharedMemorySize, C4::LDS_BYTES);

  ln1_kernel<<<256, 256, 0, stream>>>(x, xn, wqkv, wo, sumx, rsw);
  gemm1_kernel<<<256, 256, C1::LDS_BYTES + 1024, stream>>>(xn, wqkv, pq, kvp, wg, wu);
  o_kernel<<<256, 256, 0, stream>>>(pq, kvp, o, wu);
  gemm2_kernel<<<256, 256, LDS2, stream>>>(o, wo, x, hbuf, stats, sumx, rsw, mub);
  gemm3_kernel<<<256, 256, LDS3, stream>>>(hbuf, wg, wu, stats, g, wd);
  gemm4_kernel<<<256, 256, C4::LDS_BYTES, stream>>>(g, wd, hbuf, mub, out);
}
```

```cpp
#include <hip/hip_runtime.h>
#include <hip/hip_bf16.h>
#include <stdint.h>
#include <type_traits>
#include <utility>

#define LAS __attribute__((address_space(3)))
typedef unsigned short u16;
typedef __bf16 bf16x4 __attribute__((ext_vector_type(4)));
typedef __bf16 bf16x8 __attribute__((ext_vector_type(8)));
typedef float f32x4 __attribute__((ext_vector_type(4)));
typedef float f32x2 __attribute__((ext_vector_type(2)));
typedef unsigned u32x4 __attribute__((ext_vector_type(4)));
typedef unsigned u32x2 __attribute__((ext_vector_type(2)));
typedef __bf16 bf16x2 __attribute__((ext_vector_type(2)));

#define SPE 0
#define SPL 0
#define SPB 8
#define XS1 64
#define XS3 32
#define XT1 4
#define XT3 12
#define ZC 1
constexpr int S_ = 2048, D_ = 1024, F_ = 3584;

__device__ __forceinline__ unsigned pack_bf16(float lo, float hi) {
  bf16x2 v; v[0] = (__bf16)lo; v[1] = (__bf16)hi;
  return __builtin_bit_cast(unsigned, v);
}
__device__ __forceinline__ float bf_lo(unsigned u) { return __builtin_bit_cast(float, u << 16); }
__device__ __forceinline__ float bf_hi(unsigned u) { return __builtin_bit_cast(float, u & 0xffff0000u); }

#define WAIT_V(n) asm volatile("s_waitcnt vmcnt(%0)" ::"i"(n) : "memory")
#define WAIT_L0() asm volatile("s_waitcnt lgkmcnt(0)" ::: "memory")
#define BAR() asm volatile("s_barrier" ::: "memory")
#define SB() __builtin_amdgcn_sched_barrier(0)
template <class T> __device__ __forceinline__ void PIN(T& x) { asm volatile("" : "+v"(x)); }

#define TSDECL
#define TSARG
#define TSPASS
#define TSTAMP(s)
#define TSOUT(k, n)

template <int... I, class Fn>
__device__ __forceinline__ void sfor_impl(std::integer_sequence<int, I...>, Fn&& f) { (f(std::integral_constant<int, I>{}), ...); }
template <int N, class Fn>
__device__ __forceinline__ void sfor(Fn&& f) { sfor_impl(std::make_integer_sequence<int, N>{}, f); }

__device__ __forceinline__ u32x4 mk_srd(const void* p, unsigned bytes) {
  const unsigned long long a = (unsigned long long)p;
  u32x4 r;
  r[0] = __builtin_amdgcn_readfirstlane((unsigned)a);
  r[1] = __builtin_amdgcn_readfirstlane((unsigned)(a >> 32));
  r[2] = bytes;
  r[3] = 0x00020000u;
  return r;
}
__device__ __forceinline__ void glds16(const u32x4& srd, unsigned lds_dst, unsigned voff, unsigned soff) {
  asm volatile("s_mov_b32 m0, %0\n\ts_nop 0\n\tbuffer_load_dwordx4 %1, %2, %3 offen lds" ::"s"(lds_dst), "v"(voff), "s"(srd), "s"(soff) : "memory");
}
template <int IMM, class T>
__device__ __forceinline__ void gld16(T& dst, const u32x4& srd, unsigned voff, unsigned soff) {
  asm volatile("buffer_load_dwordx4 %0, %1, %2, %3 offen offset:%4" : "=v"(dst) : "v"(voff), "s"(srd), "s"(soff), "i"(IMM) : "memory");
}
template <int IMM, class T>
__device__ __forceinline__ void gld8(T& dst, const u32x4& srd, unsigned voff, unsigned soff) {
  asm volatile("buffer_load_dwordx2 %0, %1, %2, %3 offen offset:%4" : "=v"(dst) : "v"(voff), "s"(srd), "s"(soff), "i"(IMM) : "memory");
}
template <int IMM>
__device__ __forceinline__ void xst16(const f32x4& v, const u32x4& srd, unsigned voff, unsigned soff) {
  asm volatile("buffer_store_dwordx4 %0, %1, %2, %3 offen offset:%4 sc1" ::"a"(v), "v"(voff), "s"(srd), "s"(soff), "i"(IMM) : "memory");
}
template <int IMM>
__device__ __forceinline__ void xld16(f32x4& v, const u32x4& srd, unsigned voff, unsigned soff) {
  asm volatile("buffer_load_dwordx4 %0, %1, %2, %3 offen offset:%4 sc1" : "=v"(v) : "v"(voff), "s"(srd), "s"(soff), "i"(IMM) : "memory");
}
__device__ __forceinline__ void gld4(float& dst, const u32x4& srd, unsigned voff, unsigned soff) {
  asm volatile("buffer_load_dword %0, %1, %2, %3 offen" : "=v"(dst) : "v"(voff), "s"(srd), "s"(soff) : "memory");
}
__device__ __forceinline__ void glds4(const u32x4& srd, unsigned lds_dst, unsigned voff, unsigned soff) {
  asm volatile("s_mov_b32 m0, %0\n\ts_nop 0\n\tbuffer_load_dword %1, %2, %3 offen lds" ::"s"(lds_dst), "v"(voff), "s"(srd), "s"(soff) : "memory");
}
__device__ __forceinline__ void sld8(unsigned long long& sink, unsigned long long base, unsigned off) {
  asm volatile("s_load_dwordx2 %0, %1, %2" : "+s"(sink) : "s"(base), "s"(off) : "memory");
}
__device__ __forceinline__ bf16x8 tr_frag(LAS char* p0, LAS char* p1) {
  const bf16x4 lo = __builtin_amdgcn_ds_read_tr16_b64_v4bf16((LAS bf16x4*)p0);
  const bf16x4 hi = __builtin_amdgcn_ds_read_tr16_b64_v4bf16((LAS bf16x4*)p1);
  return __builtin_shufflevector(lo, hi, 0, 1, 2, 3, 4, 5, 6, 7);
}
template <bool WT> __device__ __forceinline__ void gst16(void* p, const u32x4& v) {
  if (WT) asm volatile("global_store_dwordx4 %0, %1, off sc1\n\ts_nop 1" ::"v"(p), "v"(v) : "memory");
  else *(u32x4*)p = v;
}
template <bool WT> __device__ __forceinline__ void gst8(void* p, const u32x2& v) {
  if (WT) asm volatile("global_store_dwordx2 %0, %1, off sc1\n\ts_nop 1" ::"v"(p), "v"(v) : "memory");
  else *(u32x2*)p = v;
}
#define MFMA(a, b, c) __builtin_amdgcn_mfma_f32_16x16x32_bf16(a, b, c, 0, 0, 0)
__device__ __forceinline__ void mfma_acc(f32x4& c, const bf16x8& a, const bf16x8& b) {
  asm volatile("v_mfma_f32_16x16x32_bf16 %0, %1, %2, %0" : "+a"(c) : "v"(a), "v"(b));
}
__device__ __forceinline__ void mfma_acc_va(f32x4& c, const bf16x8& a, const bf16x8& b) {
  asm volatile("v_mfma_f32_16x16x32_bf16 %0, %1, %2, %0" : "+a"(c) : "v"(a), "a"(b));
}
__device__ __forceinline__ void mfma_acc_av(f32x4& c, const bf16x8& a, const bf16x8& b) {
  asm volatile("v_mfma_f32_16x16x32_bf16 %0, %1, %2, %0" : "+a"(c) : "a"(a), "v"(b));
}
template <bool AG0, bool AG1, bool Z>
__device__ __forceinline__ void mfma_x(f32x4& c, const bf16x8& a, const bf16x8& b) {
  if constexpr (!Z) {
    if constexpr (AG0) mfma_acc_av(c, a, b); else if constexpr (AG1) mfma_acc_va(c, a, b); else mfma_acc(c, a, b);
  } else if constexpr (AG0) asm volatile("v_mfma_f32_16x16x32_bf16 %0, %1, %2, 0" : "=&a"(c) : "a"(a), "v"(b));
  else if constexpr (AG1) asm volatile("v_mfma_f32_16x16x32_bf16 %0, %1, %2, 0" : "=&a"(c) : "v"(a), "a"(b));
  else asm volatile("v_mfma_f32_16x16x32_bf16 %0, %1, %2, 0" : "=&a"(c) : "v"(a), "v"(b));
}
__device__ __forceinline__ void gld16a(bf16x8& dst, const u32x4& srd, unsigned voff, unsigned soff) {
  asm volatile("buffer_load_dwordx4 %0, %1, %2, %3 offen" : "=a"(dst) : "v"(voff), "s"(srd), "s"(soff) : "memory");
}
template <bool AG> __device__ __forceinline__ void PINR(bf16x8& x) {
  if (AG) asm volatile("" : "+a"(x)); else asm volatile("" : "+v"(x));
}
__device__ __forceinline__ void PINA(f32x4& c) { asm volatile("" : "+a"(c)); }

template <int MF, int KS, int BI, int ST, int AD, int XR, int PRO, int XE, bool LW>
struct Sched {
  static constexpr int NS = 4 * AD, BP = (BI + KS - 1) / KS, MAXX = 64;
  int total[NS + 1] = {};
  int posA[NS + AD + 1] = {}, posB[NS + ST + 2] = {};
  int nx[AD] = {};
  int xkind[AD][MAXX] = {}, xidx[AD][MAXX] = {}, xpc[AD][MAXX] = {};
  int npro = 0, pkind[MAXX] = {}, pidx[MAXX] = {}, ppc[MAXX] = {};
  constexpr Sched() {
    int n = 0;
    int qk[512] = {}, qi[512] = {}, qp[512] = {}, nq = 0, hq = 0;
    for (int p = 0; p < BI; ++p) { qk[nq] = 1; qi[nq] = 0; qp[nq] = p; ++nq; }
    for (int a = 0; a < AD || (a + 1) / KS < ST; ++a) {
      if (!LW && (a + 1) % KS == 0 && (a + 1) / KS < ST) for (int p = 0; p < BI; ++p) { qk[nq] = 1; qi[nq] = (a + 1) / KS; qp[nq] = p; ++nq; }
      if (a < AD) for (int m = 0; m < MF; ++m) { qk[nq] = 0; qi[nq] = a; qp[nq] = m; ++nq; }
    }
    while (hq < nq && hq < PRO) {
      pkind[hq] = qk[hq]; pidx[hq] = qi[hq]; ppc[hq] = qp[hq];
      ++n; if (qk[hq]) posB[qi[hq]] = n; else posA[qi[hq]] = n;
      ++hq;
    }
    npro = hq;
    for (int j = 0; j < NS; ++j) {
      total[j] = n;
      if (j < AD) {
        int c = 0;
        while (hq < nq && (c < XR || j == AD - 1)) {
          xkind[j][c] = qk[hq]; xidx[j][c] = qi[hq]; xpc[j][c] = qp[hq];
          ++n; if (qk[hq]) posB[qi[hq]] = n; else posA[qi[hq]] = n;
          ++hq; ++c;
        }
        nx[j] = c;
      }
      if (XE && j >= AD && j % XE == 0) ++n;
      if (j >= 1) { n += MF; posA[j - 1 + AD] = n; }
      if (!LW && j + 1 >= KS) {
        const int g = (j + 1) % KS, T = (j + 1) / KS - 1 + ST;
        for (int p = g * BP; p < (g + 1) * BP && p < BI; ++p) { ++n; posB[T] = n; }
      }
    }
    total[NS] = n;
  }
  constexpr int clampv(int v) const { return v > 63 ? 63 : v; }
  constexpr int waitA(int j) const { return clampv(total[j] - posA[j]); }
  constexpr int waitB(int j) const { return LW ? 63 : clampv(total[j] - posB[j / KS + 1]); }
  constexpr int min3(int a, int b, int c) const { return a < b ? (a < c ? a : c) : (b < c ? b : c); }
  constexpr int loopA(int r) const { return min3(waitA(AD + r), waitA(2 * AD + r), waitA(3 * AD + r)); }
  constexpr int loopB(int r) const { return min3(waitB(AD + r), waitB(2 * AD + r), waitB(3 * AD + r)); }
  constexpr bool ok() const {
    for (int j = 0; j < NS; ++j) {
      if (posA[j] <= 0 || posA[j] > total[j]) return false;
      if (!LW && j % KS == KS - 1 && (posB[j / KS + 1] <= 0 || posB[j / KS + 1] > total[j])) return false;
    }
    for (int j = 0; j < AD; ++j) if (nx[j] > MAXX) return false;
    if (posB[0] > total[0] || PRO > MAXX || 3 * AD / KS + 1 < ST) return false;
    return true;
  }
};

template <int MODE_, int MF_, int NF_, int KS_, int ST_, int AD_, int XR_, int PRO_, int KTOT_, int KLD_, int XE_, int MU_ = 0, int LW_ = 0, int SPF_ = 0>
struct Cfg {
  static constexpr int MODE = MODE_, MF = MF_, NF = NF_, KS = KS_, ST = ST_, AD = AD_, XR = XR_, PRO = PRO_, KTOT = KTOT_, KLD = KLD_, XE = XE_, NFX = NF_ + MU_;
  static constexpr int LW = LW_;
  static constexpr int SPF = SPF_, NT = SPF_ ? 512 : (LW_ ? 320 : 256);
  static constexpr int BK = 32 * KS;
  static constexpr int ROWB = (MODE == 3) ? 224 : NF * 32;
  static constexpr int SUB = (MODE == 3) ? BK * ROWB : 0;
  static constexpr int STAGE = BK * NF * 32;
  static constexpr int BI = STAGE / 4096;
  static constexpr int LDS_BYTES = ST * STAGE;
  static_assert((XE == 0 || AD % XE == 0) && STAGE % 4096 == 0 && AD % KS == 0 && AD % 2 == 0 && (KTOT / 32) % AD == 0, "");
};

template <class C> struct SchedOf {
  using SC = Sched<C::MF, C::KS, C::BI, C::ST, C::AD, C::XR, C::PRO, C::XE, (C::LW > 0)>;
  static constexpr SC S{};
};
template <class C> __device__ __forceinline__ int b_swz(int row) {
  if (C::MODE == 3) return 0;
  if (C::MODE == 1) return ((row >> 1) & 1) | (((row >> 3) & 1) << 1);
  if (C::ROWB % 256 == 0) return row & 7;
  return (row >> 1) & 3;
}

struct NoExt { template <class... T> __device__ __forceinline__ void operator()(T&&...) const {} };
template <class C, int NEPI, class Mid, class Ext>
__device__ __forceinline__ bool gemm_main(f32x4 (&acc)[C::MF][C::NFX], LAS char* lds, const u16* A, const u16* B0, const u16* B1, unsigned b_bytes,
                                          int ldb, const u32x4& swarm, const u32x4& spf, Mid&& mid, Ext&& ext TSARG, int cswap = 0) {
  constexpr int MF = C::MF, NF = C::NFX, KS = C::KS, ST = C::ST, AD = C::AD, BI = C::BI, ROWB = C::ROWB, NMM = MF * NF;
  using SO = SchedOf<C>;
  static_assert(SO::S.ok(), "schedule");
  constexpr int BP = SO::SC::BP;
  TSTAMP(0);
  const int lane = threadIdx.x & 63;
  const int wave = __builtin_amdgcn_readfirstlane(threadIdx.x >> 6);
  const unsigned lds0 = (unsigned)(size_t)lds;
  const u32x4 sa = mk_srd(A + (size_t)wave * MF * 16 * C::KLD, (unsigned)((MF - 1) * 16 * C::KLD + 16 * C::KTOT) * 2);
  const u32x4 sb = mk_srd((C::MODE == 3 && wave >= 2) ? B1 : B0, b_bytes);
  unsigned a_off[MF];
#pragma unroll
  for (int m = 0; m < MF; ++m) a_off[m] = lane * 16 + m * (C::KLD * 32);
  const unsigned b_tile = (unsigned)(C::BK * ldb * 2);
  auto src_of = [&](int piece) -> unsigned {
    if (C::MODE == 3) {
      const int ch = (piece % (2 * C::BI)) * 64 + lane, row = ch / 14, c = ch % 14;
      return (unsigned)(row * ldb * 2 + c * 16);
    }
    constexpr int CPR = ROWB / 16;
    const int ch = piece * 64 + lane, row = ch / CPR, cs = ch % CPR, c = cs ^ (b_swz<C>(row) << 1);
    const int col = (C::MODE == 1) ? (c >> 3) * 1024 + (c & 7) * 8 : (c ^ cswap) * 8;
    return (unsigned)(row * ldb * 2 + col * 2);
  };
  if constexpr (C::SPF > 0) {
    if (wave > 4) {
      const unsigned long long base = ((unsigned long long)spf[1] << 32) | spf[0];
      unsigned long long sink = 0;
      unsigned off = (wave - 5) * 128 + 60;
      const unsigned last = spf[2] - 68;
      auto burst = [&](auto NC) {
#pragma unroll
        for (int i = 0; i < decltype(NC)::value; ++i) sld8(sink, base, off + i * 384 < last ? off + i * 384 : last);
        off += decltype(NC)::value * 384;
      };
      burst(std::integral_constant<int, SPB>{});
      BAR();
#pragma unroll 1
      for (int t = 0; t < C::KTOT / C::BK; ++t) {
        if (off <= last) burst(std::integral_constant<int, C::SPF>{});
        BAR();
      }
      if (off <= last) burst(std::integral_constant<int, SPE>{});
      sfor<NEPI>([&](auto) { BAR(); });
      asm volatile("s_waitcnt lgkmcnt(0)" : "+s"(sink)::"memory");
      return false;
    }
  }
  if constexpr (C::LW > 0) {
    if (wave == 4) {
      constexpr int NP = 4 * C::BI, INF = C::LW;
      static_assert(INF >= 2 && INF < ST && INF * NP <= 63, "");
      unsigned src[NP];
#pragma unroll
      for (int i = 0; i < NP; ++i) src[i] = src_of(i);
      auto tile = [&](int slot, unsigned t) {
        sfor<NP>([&](auto i) { glds16(sb, lds0 + slot * C::STAGE + decltype(i)::value * 1024, src[decltype(i)::value], t * b_tile); });
      };
      tile(1, 1);
      BAR();
      sfor<INF - 2>([&](auto t) { tile(decltype(t)::value + 2, decltype(t)::value + 2); });
      int slot = INF;
      unsigned long long sink = 0;
#pragma unroll 1
      for (int t = 0; t < C::KTOT / C::BK; ++t) {
        tile(slot, t + INF);
        slot = (slot + 1 == ST) ? 0 : slot + 1;
        if constexpr (C::SPF > 0) {
          constexpr unsigned o0 = 3 * (SPB + C::KTOT / C::BK * C::SPF + SPE) * 128 + 60;
#pragma unroll
          for (int i = 0; i < SPL; ++i) {
            const unsigned o = o0 + (t * SPL + i) * 128;
            if (o + 8 <= spf[2]) sld8(sink, ((unsigned long long)spf[1] << 32) | spf[0], o);
          }
        }
        WAIT_V((INF - 1) * NP);
        BAR();
      }
      WAIT_V(0);
      sfor<NEPI>([&](auto) { BAR(); });
      asm volatile("s_waitcnt lgkmcnt(0)" : "+s"(sink)::"memory");
      return false;
    }
  }
  unsigned b_src[BI];
#pragma unroll
  for (int i = 0; i < BI; ++i) b_src[i] = src_of(wave * BI + i);
  const unsigned b_dst = lds0 + wave * BI * 1024;

  const int G = lane >> 4, kr = (lane & 15) >> 2, p = lane & 3;
  const int r0 = (C::MODE == 1 ? 8 : 4) * G + kr;
  constexpr int RD2 = (C::MODE == 1 ? 4 : 16) * ROWB;
  constexpr int NV = (C::MODE == 3) ? 1 : (ROWB % 256 == 0 ? 8 : 4);
  int b_rd[NV];
#pragma unroll
  for (int v = 0; v < NV; ++v) b_rd[v] = r0 * ROWB + ((v ^ b_swz<C>(r0)) << 5) + p * 8;
  LAS char* xfr = lds + C::LDS_BYTES + (lane & 1) * 64 + G * 16;
  auto half = [&](LAS char* base, int n, int s, int hf, int step) -> bf16x4 {
    if (n == C::NF) return *(const LAS bf16x4*)(xfr + step * 128 + hf * 8);
    int off;
    if (C::MODE == 3) off = b_rd[0] + (n & 1) * C::SUB + (n >> 1) * 32;
    else off = b_rd[n % NV] + (n / NV) * (NV * 32);
    off += s * 32 * ROWB + hf * RD2;
    return __builtin_amdgcn_ds_read_tr16_b64_v4bf16((LAS bf16x4*)(base + off));
  };

  bf16x8 ar[AD][MF];
  bf16x4 bf[2][NF][2];
  auto issueA = [&](auto RC, auto MC, unsigned step) {
    const unsigned so = step < C::KTOT / 32 ? step * 1024 : 0x7ff00000u;
    if constexpr (C::LW > 0) gld16a(ar[decltype(RC)::value][decltype(MC)::value], sa, a_off[decltype(MC)::value], so);
    else gld16<0>(ar[decltype(RC)::value][decltype(MC)::value], sa, a_off[decltype(MC)::value], so);
  };
  if constexpr (C::NFX > C::NF) glds16(swarm, lds0 + C::LDS_BYTES + wave * 1024, lane * 16, wave * 1024);
  sfor<SO::S.npro>([&](auto LC) {
    constexpr int l = decltype(LC)::value, kind = SO::S.pkind[l], idx = SO::S.pidx[l], pc = SO::S.ppc[l];
    if constexpr (kind) glds16(sb, b_dst + idx * C::STAGE + pc * 1024, b_src[pc], idx * b_tile);
    else issueA(std::integral_constant<int, idx>{}, std::integral_constant<int, pc>{}, idx);
  });
#pragma unroll
  for (int m = 0; m < MF; ++m)
#pragma unroll
    for (int n = 0; n < NF; ++n) if (!ZC) { acc[m][n] = f32x4{0.f, 0.f, 0.f, 0.f}; PINA(acc[m][n]); }
  TSTAMP(1);
  { constexpr int w0 = SO::S.total[0] - SO::S.posB[0]; WAIT_V(w0); }
  BAR();
  TSTAMP(2);
#pragma unroll
  for (int n = 0; n < NF; ++n) { bf[0][n][0] = half(lds, n, 0, 0, 0); bf[0][n][1] = half(lds, n, 0, 1, 0); }
  SB();

  int slot = 0, fslot = 0;
  auto step = [&](auto JC, auto FC, int j0) {
    constexpr int R = decltype(JC)::value;
    constexpr bool FIRST = decltype(FC)::value;
    constexpr int s = R % KS, cur = R & 1, nxt = cur ^ 1;
    constexpr int WA = FIRST ? SO::S.waitA(R) : SO::S.loopA(R), WB = (R % KS != KS - 1) ? 63 : (FIRST ? SO::S.waitB(R) : SO::S.loopB(R));
    WAIT_V(WA);
    sfor<MF>([&](auto m) { PINR<(C::LW > 0)>(ar[R][decltype(m)::value]); });
    if constexpr (s == KS - 1) {
      WAIT_L0();
      if constexpr (WB < WA) WAIT_V(WB);
      BAR();
      fslot = slot;
      slot = (slot + 1 == ST) ? 0 : slot + 1;
    }
    LAS char* rbase = lds + slot * C::STAGE;
    constexpr int ns = (s + 1) % KS;
    constexpr bool HASB = !(FIRST && R + 1 < KS) && C::LW == 0;
    constexpr int g = (R + 1) % KS;
    constexpr int NB = HASB ? ((g + 1) * BP <= BI ? BP : (BI - g * BP > 0 ? BI - g * BP : 0)) : 0;
    constexpr int NX = FIRST ? SO::S.nx[R] : (C::XE && R % (C::XE ? C::XE : 1) == 0 ? 1 : 0);
    constexpr int NA = (FIRST && R == 0) ? 0 : MF, RP = (R + AD - 1) % AD;
    constexpr int NL = NX + NA + NB;
    const unsigned gdst = b_dst + fslot * C::STAGE;
    const unsigned gsoff = (unsigned)((j0 + R + 1) / KS - 1 + ST) * b_tile;
    SB();
    sfor<NMM>([&](auto IC) {
      constexpr int i = decltype(IC)::value, n = i / MF, m = i % MF;
      const bf16x8 bb = __builtin_shufflevector(bf[cur][n][0], bf[cur][n][1], 0, 1, 2, 3, 4, 5, 6, 7);
      constexpr bool Z = ZC && FIRST && R == 0;
      if constexpr (C::MODE == 1 && n >= 4) mfma_x<(C::LW > 0), false, Z>(acc[m][n], ar[R][m], bb);
      else mfma_x<false, (C::LW > 0), Z>(acc[m][n], bb, ar[R][m]);
      SB();
      constexpr int SPAN = (NMM * 7) / 8 >= 2 * NF ? (NMM * 7) / 8 : NMM;
      sfor<2 * NF>([&](auto NC) {
        constexpr int nn = decltype(NC)::value;
        if constexpr ((nn * SPAN) / (2 * NF) == i) bf[nxt][nn >> 1][nn & 1] = half(rbase, nn >> 1, ns, nn & 1, j0 + R + 1);
      });
      sfor<NL>([&](auto LC) {
        constexpr int l = decltype(LC)::value;
        if constexpr (((2 * l + 1) * NMM) / (2 * (NL ? NL : 1)) == i) {
          if constexpr (l < NX && !FIRST) {
            constexpr int XS = C::MODE == 1 ? XS1 : XS3, XT = C::MODE == 1 ? XT1 : XT3;
            glds4(swarm, lds0 + C::LDS_BYTES + wave * 256, lane * XS, j0 + R >= XT ? (unsigned)(j0 + R - XT) * (64 * XS / (C::XE ? C::XE : 1)) : 0x7ff00000u);
          } else if constexpr (l < NX) {
            constexpr int kind = SO::S.xkind[R][l], idx = SO::S.xidx[R][l], pc = SO::S.xpc[R][l];
            if constexpr (kind) glds16(sb, b_dst + idx * C::STAGE + pc * 1024, b_src[pc], idx * b_tile);
            else issueA(std::integral_constant<int, idx>{}, std::integral_constant<int, pc>{}, idx);
          } else if constexpr (l < NX + NA) {
            issueA(std::integral_constant<int, RP>{}, std::integral_constant<int, l - NX>{}, (unsigned)(j0 + R - 1 + AD));
          } else {
            constexpr int pc = g * BP + (l - NX - NA);
            glds16(sb, gdst + pc * 1024, b_src[pc], gsoff);
          }
        }
      });
      SB();
    });
  };
  sfor<AD>([&](auto r) { step(r, std::true_type{}, 0); });
  mid();
  SB();
#pragma unroll 1
  for (int j0 = AD; j0 < C::KTOT / 32; j0 += AD) sfor<AD>([&](auto r) { step(r, std::false_type{}, j0); });
  asm volatile("s_nop 15\n\ts_nop 15" ::: "memory");
#pragma unroll
  for (int m = 0; m < MF; ++m)
#pragma unroll
    for (int n = 0; n < NF; ++n) PINA(acc[m][n]);
  TSTAMP(3);
  ext();
  WAIT_V(0);
  sfor<AD>([&](auto r) { sfor<MF>([&](auto m) { PINR<(C::LW > 0)>(ar[decltype(r)::value][decltype(m)::value]); }); });
  WAIT_L0();
  TSTAMP(5);
  return true;
}
#define C1_SPF 6
#define C1_ST 4
#define C1_AD 4
#define C1_XR 6
#define C2_ST 4
#define C2_XR 3
#define C3_ST 4
#define C3_XR 10
#define C4_ST 4
#define C4_XR 4
#define C1_PRO 16
#define C2_PRO 16
#define C3_PRO 18
#define C4_PRO 16
#define MAP4 0
#define WTALL 1
constexpr bool WT0 = WTALL, WT1 = WTALL, WTO = WTALL, WT2 = WTALL, WT3 = true, WT4 = WTALL;
#define PFMODE 2
#define C1_XE (C1_SPF ? 0 : 4)
#define C3_XE 4
#define C1_LW 2
#define C2_LW 3
#define C4_LW 3
#define EPQ 2
#define PFR 704
#define C2_SPF 0
#define PFRO PFR
#define PFR2 PFRO
static_assert(PFR % 16 == 0 && PFRO % 16 == 0 && PFR2 % 16 == 0 && (C2_SPF == 0 || PFR2 > PFRO) && PFR <= PFRO && PFRO <= PFR2 && PFR2 <= 1024 && (PFRO - PFR) * 14 <= 8192 && (1024 - PFR2) * 7 <= 4096, "");
#define LNR 1024
#define ALIAS 2

__global__ __launch_bounds__(256) void ln1_kernel(const u16* __restrict__ x, u16* __restrict__ y, const u16* __restrict__ wqkv,
                                                  const u16* __restrict__ wo, float* __restrict__ sumx, u16* __restrict__ rsw,
                                                  unsigned* __restrict__ flags) {
  __shared__ float red[2][4][8];
  const int t = threadIdx.x, wave = t >> 6, r = t & 7, c0 = t >> 3, row = blockIdx.x * 8 + r;
  u32x4 v[4], pf[6];
  const u32x4 sx = mk_srd(x + (size_t)blockIdx.x * 8 * D_, 8 * D_ * 2);
  sfor<4>([&](auto i) { gld16<decltype(i)::value * 512>(v[decltype(i)::value], sx, r * D_ * 2 + c0 * 16, 0); });
  const u32x4 s1 = mk_srd(wqkv + (size_t)blockIdx.x * (LNR * 12), LNR * 24);
  sfor<6>([&](auto i) { gld16<0>(pf[decltype(i)::value], s1, t * 16, decltype(i)::value * 4096); });
  u32x4 wr[2];
  const u32x4 s2 = mk_srd(wo + (size_t)blockIdx.x * 4096, 8192);
  gld16<0>(wr[0], s2, t * 32, 0);
  gld16<16>(wr[1], s2, t * 32, 0);
  WAIT_V(8);
  sfor<4>([&](auto i) { PIN(v[decltype(i)::value]); });
  float s = 0.f;
#pragma unroll
  for (int i = 0; i < 4; ++i)
#pragma unroll
    for (int j = 0; j < 4; ++j) s += bf_lo(v[i][j]) + bf_hi(v[i][j]);
  s += __shfl_xor(s, 8); s += __shfl_xor(s, 16); s += __shfl_xor(s, 32);
  if ((t & 63) < 8) red[0][wave][r] = s;
  __syncthreads();
  const float sx4 = red[0][0][r] + red[0][1][r] + red[0][2][r] + red[0][3][r];
  const float m = sx4 * (1.f / D_);
  if (t < 8) sumx[row] = sx4;
  if (t == 8) flags[blockIdx.x * 32] = 0u;
  float q = 0.f;
#pragma unroll
  for (int i = 0; i < 4; ++i)
#pragma unroll
    for (int j = 0; j < 4; ++j) { const float a = bf_lo(v[i][j]) - m, b = bf_hi(v[i][j]) - m; q += a * a + b * b; }
  q += __shfl_xor(q, 8); q += __shfl_xor(q, 16); q += __shfl_xor(q, 32);
  if ((t & 63) < 8) red[1][wave][r] = q;
  __syncthreads();
  const float var = (red[1][0][r] + red[1][1][r] + red[1][2][r] + red[1][3][r]) * (1.f / D_);
  const float rs = 1.f / sqrtf(var + 1e-5f);
#pragma unroll
  for (int i = 0; i < 4; ++i) {
    u32x4 o;
#pragma unroll
    for (int j = 0; j < 4; ++j) o[j] = pack_bf16((bf_lo(v[i][j]) - m) * rs, (bf_hi(v[i][j]) - m) * rs);
    const int c = c0 + 32 * i;
    gst16<WT0>(y + ((size_t)((row >> 4) * 32 + (c >> 2)) * 64 + (c & 3) * 16 + (row & 15)) * 8, o);
  }
  WAIT_V(0);
  sfor<6>([&](auto i) { PIN(pf[decltype(i)::value]); });
  PIN(wr[0]); PIN(wr[1]);
  float ws = 0.f;
#pragma unroll
  for (int j = 0; j < 4; ++j) ws += (bf_lo(wr[0][j]) + bf_hi(wr[0][j])) + (bf_lo(wr[1][j]) + bf_hi(wr[1][j]));
#pragma unroll
  for (int d = 1; d < 64; d <<= 1) ws += __shfl_xor(ws, d);
  if ((t & 63) == 0) {
    const int k = blockIdx.x * 4 + wave, kk = k & 31;
    const unsigned hi = pack_bf16(ws, 0.f), lo = pack_bf16(ws - bf_lo(hi), 0.f);
    u16* dst = rsw + (k >> 5) * 64 + ((kk & 15) >> 2) * 8 + (kk & 3) + (kk >> 4) * 4;
    dst[0] = (u16)hi;
    dst[32] = (u16)lo;
  }
}

__device__ __forceinline__ float phi(float t) {
  const float e = __builtin_amdgcn_fmed3f(__builtin_amdgcn_exp2f(t * 1.4426950408889634f), 0.f, 1.f);
  return (fmaxf(t, 0.f) + e) + 1e-6f;
}
__device__ __forceinline__ bf16x8 pack8(const f32x4& a, const f32x4& b) {
  u32x4 o = {pack_bf16(a[0], a[1]), pack_bf16(a[2], a[3]), pack_bf16(b[0], b[1]), pack_bf16(b[2], b[3])};
  return __builtin_bit_cast(bf16x8, o);
}
__device__ __forceinline__ f32x4 phi4(const f32x4& a) { return f32x4{phi(a[0]), phi(a[1]), phi(a[2]), phi(a[3])}; }

constexpr int KVP = 1024 + 32;
using C1 = Cfg<1, 2, 12, 2, C1_ST, C1_AD, C1_XR, C1_PRO, 1024, 1024, C1_XE, 0, C1_LW, C1_SPF>;
constexpr int LDS1 = C1::LDS_BYTES + 1024 + 48 * 1024;
__global__ __launch_bounds__(C1::NT, 1) void gemm1_kernel(const u16* __restrict__ xn, const u16* __restrict__ wqkv, u16* __restrict__ pq,
                                                       float* __restrict__ kvp, const u16* __restrict__ wg,
                                                       const u16* __restrict__ wu) {
  extern __shared__ __attribute__((aligned(16))) char smem[];
  LAS char* lds = (LAS char*)smem;
  const int b = blockIdx.x, xcd = b & 7, idx = b >> 3;
  const int tm = (xcd >> 2) * 8 + (idx & 7), tn = (xcd & 3) * 4 + (idx >> 3);
  f32x4 acc[2][12];
  TSDECL;
  const int lane = threadIdx.x & 63, wave = __builtin_amdgcn_readfirstlane(threadIdx.x >> 6), gw = b * 4 + wave;
  const u32x4 sw = mk_srd(b < 128 ? wg + b * (PFR * 28) : wu + (b - 128) * (PFR * 28), PFR * 56);
  if (!gemm_main<C1, 1>(acc, lds, xn + (size_t)tm * 128 * D_, wqkv + tn * 64, wqkv, (unsigned)(D_ * 3 * D_ - tn * 64) * 2, 3 * D_, sw, sw, [] {}, NoExt{} TSPASS)) return;
  LAS f32x4* red = (LAS f32x4*)(lds + C1::LDS_BYTES + 1024);
  const u32x4 one4 = {0x3f803f80u, 0x3f803f80u, 0x3f803f80u, 0x3f803f80u};
  const f32x4 z4 = {0.f, 0.f, 0.f, 0.f};
#pragma unroll
  for (int hh = 0; hh < 2; ++hh) {
    const bf16x8 v0 = pack8(acc[0][8 + 2 * hh], acc[1][8 + 2 * hh]), v1 = pack8(acc[0][9 + 2 * hh], acc[1][9 + 2 * hh]);
#pragma unroll
    for (int i = 0; i < 2; ++i) {
      const bf16x8 pk = pack8(phi4(acc[0][4 + 2 * hh + i]), phi4(acc[1][4 + 2 * hh + i]));
      red[((wave * 2 + hh) * 6 + 2 * i) * 64 + lane] = MFMA(pk, v0, z4);
      red[((wave * 2 + hh) * 6 + 2 * i + 1) * 64 + lane] = MFMA(pk, v1, z4);
      red[((wave * 2 + hh) * 6 + 4 + i) * 64 + lane] = MFMA(pk, __builtin_bit_cast(bf16x8, one4), z4);
    }
  }
  SB();
#pragma unroll
  for (int m = 0; m < 2; ++m)
#pragma unroll
    for (int hh = 0; hh < EPQ; ++hh)
      gst16<WT1>(pq + ((size_t)((tm * 8 + wave * 2 + m) * 32 + tn * 2 + hh) * 64 + lane) * 8, __builtin_bit_cast(u32x4, pack8(phi4(acc[m][2 * hh]), phi4(acc[m][2 * hh + 1]))));
  WAIT_L0();
  BAR();
#pragma unroll
  for (int e = 0; e < 3; ++e) {
    const int blk = e * 4 + wave, hh = blk / 6, bb = blk % 6;
    const f32x4 s = (red[blk * 64 + lane] + red[(12 + blk) * 64 + lane]) + (red[(24 + blk) * 64 + lane] + red[(36 + blk) * 64 + lane]);
    float* dst = kvp + ((size_t)(tn * 2 + hh) * 16 + tm) * KVP;
    if (bb < 4) gst16<WT1>(dst + bb * 256 + lane * 4, __builtin_bit_cast(u32x4, s));
    else if ((lane & 15) == 0) gst16<WT1>(dst + 1024 + (bb - 4) * 16 + (lane >> 4) * 4, __builtin_bit_cast(u32x4, s));
  }
  SB();
#pragma unroll
  for (int m = 0; m < 2; ++m)
#pragma unroll
    for (int hh = EPQ; hh < 2; ++hh)
      gst16<WT1>(pq + ((size_t)((tm * 8 + wave * 2 + m) * 32 + tn * 2 + hh) * 64 + lane) * 8, __builtin_bit_cast(u32x4, pack8(phi4(acc[m][2 * hh]), phi4(acc[m][2 * hh + 1]))));
  WAIT_V(0);
  TSTAMP(4);
  TSOUT(0, 6);
}

__global__ __launch_bounds__(256) void o_kernel(const u16* __restrict__ pq, const float* __restrict__ kvp, u16* __restrict__ o,
                                                const u16* __restrict__ wg, const u16* __restrict__ wu) {
  __shared__ __attribute__((aligned(16))) float sh[1024];
  const int t = threadIdx.x, lane = t & 63, wave = __builtin_amdgcn_readfirstlane(t >> 6);
  const int head = blockIdx.x & 31, rg = blockIdx.x >> 5;
  const u32x4 sk = mk_srd(kvp + (size_t)head * 16 * KVP, 16 * KVP * 4), sq = mk_srd(pq + ((size_t)(rg * 16 + wave * 4) * 32 + head) * 512, 100 * 1024);
  const int gw = blockIdx.x * 4 + wave;
  const u32x4 sw = mk_srd((gw < 512 ? wg : wu) + (size_t)PFR * F_ + (gw & 511) * ((PFRO - PFR) * 7), (PFRO - PFR) * 14);
  f32x4 pv[16], zp;
  bf16x8 qf[4];
  float pf[2];
  sfor<16>([&](auto i) { gld16<0>(pv[decltype(i)::value], sk, t * 16, decltype(i)::value * KVP * 4); });
  gld16<0>(zp, sk, t < 128 ? (t >> 3) * KVP * 4 + 4096 + (t & 7) * 16 : 0x7ffffff0u, 0);
  sfor<4>([&](auto m) { gld16<0>(qf[decltype(m)::value], sq, lane * 16, decltype(m)::value * 32768); });
  gld4(pf[0], sw, lane * 64, 0);
  gld4(pf[1], sw, lane * 64, 4096);
  WAIT_V(2);
  sfor<16>([&](auto i) { PIN(pv[decltype(i)::value]); });
  sfor<4>([&](auto m) { PIN(qf[decltype(m)::value]); });
  PIN(zp);
#pragma unroll
  for (int st = 8; st > 0; st >>= 1)
#pragma unroll
    for (int i = 0; i < st; ++i) pv[i] += pv[i + st];
  u32x2 pk2 = {pack_bf16(pv[0][0], pv[0][1]), pack_bf16(pv[0][2], pv[0][3])};
  *(u32x2*)((char*)sh + ((wave & 1) * 64 + lane) * 16 + (wave >> 1) * 8) = pk2;
  if (t < 128) *(f32x4*)(sh + 512 + t * 4) = zp;
  __syncthreads();
  const bf16x8 kv0 = *(const bf16x8*)((char*)sh + lane * 16), kv1 = *(const bf16x8*)((char*)sh + (64 + lane) * 16);
  f32x4 za = {0.f, 0.f, 0.f, 0.f}, zb = za;
#pragma unroll
  for (int i = 0; i < 16; ++i) {
    za += *(const f32x4*)(sh + 512 + i * 32 + (lane >> 4) * 4);
    zb += *(const f32x4*)(sh + 512 + i * 32 + 16 + (lane >> 4) * 4);
  }
  const bf16x8 zf = pack8(za, zb);
  const f32x4 z4 = {0.f, 0.f, 0.f, 0.f};
#pragma unroll
  for (int m = 0; m < 4; ++m) {
    const f32x4 n0 = MFMA(kv0, qf[m], z4), n1 = MFMA(kv1, qf[m], z4), dn = MFMA(zf, qf[m], z4);
    const float inv = 1.f / (dn[0] + 1e-6f);
    gst16<WTO>(o + ((size_t)((rg * 16 + wave * 4 + m) * 32 + head) * 64 + lane) * 8, __builtin_bit_cast(u32x4, pack8(n0 * inv, n1 * inv)));
  }
  WAIT_V(0);
  PIN(pf[0]); PIN(pf[1]);
}

using C2 = Cfg<2, 2, 4, 4, C2_ST, 8, C2_XR, C2_PRO, 1024, 1024, 0, 1, C2_LW, C2_SPF>;
constexpr int LDS2 = C2::LDS_BYTES + 4096 + 256;
__global__ __launch_bounds__(C2::NT, 1) void gemm2_kernel(const u16* __restrict__ o, const u16* __restrict__ wo, const u16* __restrict__ x,
                                                       u16* __restrict__ h, float* __restrict__ stats, const float* __restrict__ sumx,
                                                       const u16* __restrict__ rsw, float* __restrict__ mub,
                                                       const u16* __restrict__ wg, const u16* __restrict__ wu) {
  extern __shared__ __attribute__((aligned(16))) char smem[];
  LAS char* lds = (LAS char*)smem;
  const int b = blockIdx.x, xcd = b & 7, idx = b >> 3;
  const int tm = (xcd >> 1) * 4 + (idx & 3), tn = (xcd & 1) * 8 + (idx >> 2);
  const int lane = threadIdx.x & 63, wave = __builtin_amdgcn_readfirstlane(threadIdx.x >> 6);
  f32x4 acc[2][5];
  u32x2 res[2][4];
  float sxr[2];
  const u32x4 sx = mk_srd(x + (size_t)(tm * 128 + wave * 32) * D_ + tn * 64, 32 * D_ * 2);
  const u32x4 ssx = mk_srd(sumx + tm * 128 + wave * 32, 128), sr = mk_srd(rsw, 4096);
  const unsigned xo = (unsigned)((lane & 15) * D_ * 2 + (lane >> 4) * 8);
  const u32x4 sp = mk_srd((b < 128 ? wg : wu) + (size_t)PFRO * F_ + (b & 127) * ((PFR2 - PFRO) * 28), (PFR2 - PFRO) * 56);
  TSDECL;
  if (!gemm_main<C2, 0>(acc, lds, o + (size_t)tm * 128 * D_, wo + tn * 64, wo, (unsigned)(D_ * D_ - tn * 64) * 2, D_, sr, sp, [&] {
    sfor<2>([&](auto m) {
      sfor<4>([&](auto n) { gld8<decltype(n)::value * 32>(res[decltype(m)::value][decltype(n)::value], sx, xo, decltype(m)::value * 16 * D_ * 2); });
      gld4(sxr[decltype(m)::value], ssx, (lane & 15) * 4, decltype(m)::value * 64);
    });
  }, NoExt{} TSPASS)) return;
#pragma unroll
  for (int m = 0; m < 2; ++m) {
    unsigned w[8];
    PIN(sxr[m]);
    const float mu = (sxr[m] + (acc[m][4][0] + acc[m][4][1])) * (1.f / D_);
#pragma unroll
    for (int n = 0; n < 4; ++n) {
      PIN(res[m][n]);
      w[2 * n] = pack_bf16(bf_lo(res[m][n][0]) + acc[m][n][0] - mu, bf_hi(res[m][n][0]) + acc[m][n][1] - mu);
      w[2 * n + 1] = pack_bf16(bf_lo(res[m][n][1]) + acc[m][n][2] - mu, bf_hi(res[m][n][1]) + acc[m][n][3] - mu);
    }
    const int mb = tm * 8 + wave * 2 + m;
    gst16<WT2>(h + ((size_t)(mb * 32 + tn * 2) * 64 + lane) * 8, u32x4{w[0], w[1], w[2], w[3]});
    gst16<WT2>(h + ((size_t)(mb * 32 + tn * 2 + 1) * 64 + lane) * 8, u32x4{w[4], w[5], w[6], w[7]});
    float q = 0.f;
#pragma unroll
    for (int i = 0; i < 8; ++i) { const float a = bf_lo(w[i]), c = bf_hi(w[i]); q += a * a + c * c; }
    q += __shfl_xor(q, 16); q += __shfl_xor(q, 32);
    if (lane < 16) {
      stats[(size_t)(mb * 16 + lane) * 16 + tn] = q;
      if (tn == 0) mub[mb * 16 + lane] = mu;
    }
  }
  TSTAMP(4);
  TSOUT(1, 6);
}

using C3 = Cfg<3, 4, 14, 2, C3_ST, 4, C3_XR, C3_PRO, 1024, 1024, C3_XE>;
constexpr int LDS3 = C3::LDS_BYTES + 1024;
template <int PAR>
__device__ __forceinline__ void geglu_store(f32x4 (&acc)[4][14], const float (&var)[4], u16* g, int mb0, int tn, int lane) {
#pragma unroll
  for (int m = 0; m < 4; ++m) {
    const float ir2 = var[m], r = __builtin_amdgcn_rsqf(ir2), k1 = -2.f * 0.7978845608028654f * 1.4426950408889634f * r, k2 = k1 * 0.044715f * r * r;
    char* dst = (char*)g + ((size_t)(mb0 + m) * (F_ / 32) + ((tn * 7) >> 1)) * 1024;
    unsigned w[14];
#pragma unroll
    for (int n = 0; n < 7; ++n) {
      const f32x4 a = acc[m][2 * n], u = acc[m][2 * n + 1];
      const f32x4 arg = a * (a * a * k2 + k1);
      f32x4 d;
#pragma unroll
      for (int i = 0; i < 4; ++i) d[i] = __builtin_amdgcn_exp2f(arg[i]);
      d = d * ir2 + ir2;
#pragma unroll
      for (int i = 0; i < 4; ++i) d[i] = __builtin_amdgcn_rcpf(d[i]);
      const f32x4 o = a * u * d;
      w[2 * n] = pack_bf16(o[0], o[1]);
      w[2 * n + 1] = pack_bf16(o[2], o[3]);
    }
    if (PAR == 0) {
#pragma unroll
      for (int i = 0; i < 3; ++i) gst16<WT3>(dst + i * 1024 + lane * 16, u32x4{w[4 * i], w[4 * i + 1], w[4 * i + 2], w[4 * i + 3]});
      gst8<WT3>(dst + 3 * 1024 + lane * 16, u32x2{w[12], w[13]});
    } else {
      gst8<WT3>(dst + lane * 16 + 8, u32x2{w[0], w[1]});
#pragma unroll
      for (int i = 0; i < 3; ++i) gst16<WT3>(dst + (i + 1) * 1024 + lane * 16, u32x4{w[4 * i + 2], w[4 * i + 3], w[4 * i + 4], w[4 * i + 5]});
    }
  }
}
__global__ __launch_bounds__(256, 1) void gemm3_kernel(const u16* __restrict__ h, const u16* __restrict__ wg, const u16* __restrict__ wu,
                                                       const float* __restrict__ stats, u16* __restrict__ g,
                                                       const u16* __restrict__ wd) {
  extern __shared__ __attribute__((aligned(16))) char smem[];
  LAS char* lds = (LAS char*)smem;
  const int b = blockIdx.x, xcd = b & 7, idx = b >> 3;
  const int tm = idx & 7, tn = xcd * 4 + (idx >> 3);
  const int lane = threadIdx.x & 63, wave = __builtin_amdgcn_readfirstlane(threadIdx.x >> 6);
  f32x4 acc[4][14];
  f32x4 st[4];
  const u32x4 ss = mk_srd(stats + (size_t)(tm * 256 + wave * 64) * 16, 64 * 64);
  const u32x4 sw = mk_srd(wd + (b * 4 + wave) * 3584, 7168);
  TSDECL;
  gemm_main<C3, 0>(acc, lds, h + (size_t)tm * 256 * D_, wg + tn * 112, wu + tn * 112, (unsigned)(D_ * F_ - tn * 112) * 2, F_, sw, sw, [&] {
    sfor<4>([&](auto i) { gld16<decltype(i)::value * 16>(st[decltype(i)::value], ss, lane * 64, 0); });
    {
      constexpr int HB = (1024 - PFR2) * 7;
      const int gw = b * 4 + wave;
      const u32x4 s2 = mk_srd((gw < 512 ? wg : wu) + (size_t)PFR2 * F_ + (gw & 511) * (HB / 2), 512 * HB + HB);
      const unsigned vo = lane * 64 < HB ? lane * 64 : 0x7ff00000u;
      glds4(s2, (unsigned)(size_t)lds + C3::LDS_BYTES + wave * 256, vo, 0);
      glds4(s2, (unsigned)(size_t)lds + C3::LDS_BYTES + wave * 256, vo, 512 * HB);
    }
  }, [&] {
  float var[4];
  {
    sfor<4>([&](auto i) { PIN(st[decltype(i)::value]); });
    const f32x4 q4 = (st[0] + st[1]) + (st[2] + st[3]);
    const float v = ((q4[0] + q4[1]) + (q4[2] + q4[3])) * (1.f / D_) + 1e-5f;
#pragma unroll
    for (int m = 0; m < 4; ++m) var[m] = __shfl(v, m * 16 + (lane & 15));
  }
  TSTAMP(6);
  if (tn & 1) geglu_store<1>(acc, var, g, tm * 16 + wave * 4, tn, lane);
  else geglu_store<0>(acc, var, g, tm * 16 + wave * 4, tn, lane);
  } TSPASS);
  TSTAMP(4);
  TSOUT(2, 7);
}

using C4 = Cfg<2, 2, 8, 2, C4_ST, 8, C4_XR, C4_PRO, F_ / 2, F_, 0, 0, C4_LW>;
typedef __attribute__((address_space(1))) unsigned gu32;
__global__ __launch_bounds__(C4_LW ? 320 : 256, 1) void gemm4_kernel(const u16* __restrict__ g, const u16* __restrict__ wd, const u16* __restrict__ h,
                                                       const float* __restrict__ mub, u16* __restrict__ out, float* xbuf, unsigned* flags) {
  extern __shared__ __attribute__((aligned(16))) char smem[];
  LAS char* lds = (LAS char*)smem;
  const int b = blockIdx.x, xcd = b & 7, idx = b >> 3;
  const int kh = xcd & 1, tm = ((xcd >> 1) & 1) * 8 + (idx & 7), tn2 = (xcd >> 2) * 4 + (idx >> 3), tn = tn2 * 2 + kh;
  const int lane = threadIdx.x & 63, wave = __builtin_amdgcn_readfirstlane(threadIdx.x >> 6);
  f32x4 acc[2][8];
  u32x4 res[2][2];
  float mu[2];
  const u32x4 smu = mk_srd(mub + tm * 128 + wave * 32, 128);
  const u32x4 sh = mk_srd(h + ((size_t)(tm * 8 + wave * 2) * 32 + tn * 2) * 512, 34 * 1024);
  TSDECL;
  if (!gemm_main<C4, 1>(acc, lds, g + (size_t)tm * 128 * F_ + kh * (F_ / 2 * 16), wd + (size_t)kh * (F_ / 2) * D_ + tn2 * 128, wd,
                (unsigned)(F_ / 2 * D_ - tn2 * 128) * 2, D_, sh, sh, [&] {
    sfor<2>([&](auto m) {
      gld16<0>(res[decltype(m)::value][0], sh, lane * 16, decltype(m)::value * 32768);
      gld16<1024>(res[decltype(m)::value][1], sh, lane * 16, decltype(m)::value * 32768);
      gld4(mu[decltype(m)::value], smu, (lane & 15) * 4, decltype(m)::value * 64);
    });
  }, NoExt{} TSPASS, kh * 8)) return;
  const u32x4 so = mk_srd(xbuf + (size_t)(b * 4 + wave) * 2048, 8192), si = mk_srd(xbuf + (size_t)((b ^ 1) * 4 + wave) * 2048, 8192);
  sfor<8>([&](auto IC) {
    constexpr int i = decltype(IC)::value;
    xst16<(i & 3) * 1024>(acc[i >> 2][4 + (i & 3)], so, lane * 16, (i >> 2) * 4096);
  });
  WAIT_V(0);
  BAR();
  if (threadIdx.x == 0) __hip_atomic_store((gu32*)(flags + b * 32), 1u, __ATOMIC_RELAXED, __HIP_MEMORY_SCOPE_AGENT);
  TSTAMP(6);
  for (int spin = 0; spin < (1 << 22); ++spin) {
    if (__builtin_amdgcn_readfirstlane(__hip_atomic_load((gu32*)(flags + (b ^ 1) * 32), __ATOMIC_RELAXED, __HIP_MEMORY_SCOPE_AGENT))) break;
    __builtin_amdgcn_s_sleep(1);
  }
  TSTAMP(7);
  f32x4 oth[2][4];
  sfor<8>([&](auto IC) {
    constexpr int i = decltype(IC)::value;
    xld16<(i & 3) * 1024>(oth[i >> 2][i & 3], si, lane * 16, (i >> 2) * 4096);
  });
  LAS char* patch = lds + wave * (32 * 144);
  sfor<8>([&](auto IC) {
    constexpr int i = decltype(IC)::value, m = i >> 2, n = i & 3;
    PIN(res[m][n >> 1]); PIN(mu[m]);
    const unsigned r0 = res[m][n >> 1][(n & 1) * 2], r1 = res[m][n >> 1][(n & 1) * 2 + 1];
    const f32x4 base = f32x4{bf_lo(r0), bf_hi(r0), bf_lo(r1), bf_hi(r1)} + mu[m];
    WAIT_V(7 - i);
    PIN(oth[m][n]);
    const f32x4 v = base + (kh ? oth[m][n] + acc[m][n] : acc[m][n] + oth[m][n]);
    *(LAS u32x2*)(patch + (m * 16 + (lane & 15)) * 144 + n * 32 + (lane >> 4) * 8) = u32x2{pack_bf16(v[0], v[1]), pack_bf16(v[2], v[3])};
  });
#pragma unroll
  for (int i = 0; i < 4; ++i) {
    const int row = i * 8 + (lane >> 3);
    const u32x4 w = *(const LAS u32x4*)(patch + row * 144 + (lane & 7) * 16);
    gst16<WT4>(out + (size_t)(tm * 128 + wave * 32 + row) * D_ + tn * 64 + (lane & 7) * 8, w);
  }
  TSTAMP(4);
  TSOUT(3, 8);
}

extern "C" void kernel_launch(void* const* d_in, const int* in_sizes, int n_in, void* d_out, int out_size, void* d_ws, size_t ws_size,
                              hipStream_t stream) {
  const u16* x = (const u16*)d_in[0];
  const u16* wqkv = (const u16*)d_in[1];
  const u16* wo = (const u16*)d_in[2];
  const u16* wg = (const u16*)d_in[4];
  const u16* wu = (const u16*)d_in[5];
  const u16* wd = (const u16*)d_in[6];
  u16* out = (u16*)d_out;
  char* ws = (char*)d_ws;
  const size_t MB = 1 << 20;
  u16* xn = (u16*)(ws);
  u16* pq = (u16*)(ws + 4 * MB);
  u16* o = (u16*)(ws + 8 * MB);
  u16* hbuf = (u16*)(ws + 16 * MB);
  float* kvp = (float*)(ws + 12 * MB);
  float* stats = (float*)(ws + 20 * MB);
  float* sumx = (float*)(ws + 21 * MB);
  float* mub = (float*)(ws + 21 * MB + 65536);
  u16* rsw = (u16*)(ws + 21 * MB + 131072);
  u16* g = (u16*)(ws + (ALIAS == 2 ? 0 : 24) * MB);
  unsigned* flags = (unsigned*)(ws + 22 * MB);
  float* xbuf = (float*)(ws + (ALIAS == 1 ? 0 : 40) * MB);

  (void)hipFuncSetAttribute((const void*)gemm1_kernel, hipFuncAttributeMaxDynamicSharedMemorySize, LDS1);
  (void)hipFuncSetAttribute((const void*)gemm2_kernel, hipFuncAttributeMaxDynamicSharedMemorySize, LDS2);
  (void)hipFuncSetAttribute((const void*)gemm3_kernel, hipFuncAttributeMaxDynamicSharedMemorySize, LDS3);
  (void)hipFuncSetAttribute((const void*)gemm4_kernel, hipFuncAttributeMaxDynamicSharedMemorySize, C4::LDS_BYTES);

  ln1_kernel<<<256, 256, 0, stream>>>(x, xn, wqkv, wo, sumx, rsw, flags);
  gemm1_kernel<<<256, C1::NT, LDS1, stream>>>(xn, wqkv, pq, kvp, wg, wu);
  o_kernel<<<256, 256, 0, stream>>>(pq, kvp, o, wg, wu);
  gemm2_kernel<<<256, C2::NT, LDS2, stream>>>(o, wo, x, hbuf, stats, sumx, rsw, mub, wg, wu);
  gemm3_kernel<<<256, 256, LDS3, stream>>>(hbuf, wg, wu, stats, g, wd);
  gemm4_kernel<<<256, C4_LW ? 320 : 256, C4::LDS_BYTES, stream>>>(g, wd, hbuf, mub, out, xbuf, flags);
}
```

```cpp
#include <hip/hip_runtime.h>
#include <hip/hip_bf16.h>
#include <stdint.h>
#include <type_traits>
#include <utility>

#define LAS __attribute__((address_space(3)))
typedef unsigned short u16;
typedef __bf16 bf16x4 __attribute__((ext_vector_type(4)));
typedef __bf16 bf16x8 __attribute__((ext_vector_type(8)));
typedef float f32x4 __attribute__((ext_vector_type(4)));
typedef float f32x2 __attribute__((ext_vector_type(2)));
typedef unsigned u32x4 __attribute__((ext_vector_type(4)));
typedef unsigned u32x2 __attribute__((ext_vector_type(2)));
typedef __bf16 bf16x2 __attribute__((ext_vector_type(2)));

#define SPE 0
#define SPL 0
#define SPB 8
#define XS1 64
#define XS3 32
#define XT1 4
#define XT3 12
#define ZC 1
constexpr int S_ = 2048, D_ = 1024, F_ = 3584;

__device__ __forceinline__ unsigned pack_bf16(float lo, float hi) {
  bf16x2 v; v[0] = (__bf16)lo; v[1] = (__bf16)hi;
  return __builtin_bit_cast(unsigned, v);
}
__device__ __forceinline__ float bf_lo(unsigned u) { return __builtin_bit_cast(float, u << 16); }
__device__ __forceinline__ float bf_hi(unsigned u) { return __builtin_bit_cast(float, u & 0xffff0000u); }

#define WAIT_V(n) asm volatile("s_waitcnt vmcnt(%0)" ::"i"(n) : "memory")
#define WAIT_L0() asm volatile("s_waitcnt lgkmcnt(0)" ::: "memory")
#define BAR() asm volatile("s_barrier" ::: "memory")
#define SB() __builtin_amdgcn_sched_barrier(0)
template <class T> __device__ __forceinline__ void PIN(T& x) { asm volatile("" : "+v"(x)); }

#define TSDECL
#define TSARG
#define TSPASS
#define TSTAMP(s)
#define TSOUT(k, n)

template <int... I, class Fn>
__device__ __forceinline__ void sfor_impl(std::integer_sequence<int, I...>, Fn&& f) { (f(std::integral_constant<int, I>{}), ...); }
template <int N, class Fn>
__device__ __forceinline__ void sfor(Fn&& f) { sfor_impl(std::make_integer_sequence<int, N>{}, f); }

__device__ __forceinline__ u32x4 mk_srd(const void* p, unsigned bytes) {
  const unsigned long long a = (unsigned long long)p;
  u32x4 r;
  r[0] = __builtin_amdgcn_readfirstlane((unsigned)a);
  r[1] = __builtin_amdgcn_readfirstlane((unsigned)(a >> 32));
  r[2] = bytes;
  r[3] = 0x00020000u;
  return r;
}
__device__ __forceinline__ void glds16(const u32x4& srd, unsigned lds_dst, unsigned voff, unsigned soff) {
  asm volatile("s_mov_b32 m0, %0\n\ts_nop 0\n\tbuffer_load_dwordx4 %1, %2, %3 offen lds" ::"s"(lds_dst), "v"(voff), "s"(srd), "s"(soff) : "memory");
}
template <int IMM, class T>
__device__ __forceinline__ void gld16(T& dst, const u32x4& srd, unsigned voff, unsigned soff) {
  asm volatile("buffer_load_dwordx4 %0, %1, %2, %3 offen offset:%4" : "=v"(dst) : "v"(voff), "s"(srd), "s"(soff), "i"(IMM) : "memory");
}
template <int IMM, class T>
__device__ __forceinline__ void gld8(T& dst, const u32x4& srd, unsigned voff, unsigned soff) {
  asm volatile("buffer_load_dwordx2 %0, %1, %2, %3 offen offset:%4" : "=v"(dst) : "v"(voff), "s"(srd), "s"(soff), "i"(IMM) : "memory");
}
template <int IMM>
__device__ __forceinline__ void xst16(const f32x4& v, const u32x4& srd, unsigned voff, unsigned soff) {
  asm volatile("buffer_store_dwordx4 %0, %1, %2, %3 offen offset:%4 sc1" ::"a"(v), "v"(voff), "s"(srd), "s"(soff), "i"(IMM) : "memory");
}
template <int IMM>
__device__ __forceinline__ void xld16(f32x4& v, const u32x4& srd, unsigned voff, unsigned soff) {
  asm volatile("buffer_load_dwordx4 %0, %1, %2, %3 offen offset:%4 sc1" : "=v"(v) : "v"(voff), "s"(srd), "s"(soff), "i"(IMM) : "memory");
}
__device__ __forceinline__ void gld4(float& dst, const u32x4& srd, unsigned voff, unsigned soff) {
  asm volatile("buffer_load_dword %0, %1, %2, %3 offen" : "=v"(dst) : "v"(voff), "s"(srd), "s"(soff) : "memory");
}
__device__ __forceinline__ void glds4(const u32x4& srd, unsigned lds_dst, unsigned voff, unsigned soff) {
  asm volatile("s_mov_b32 m0, %0\n\ts_nop 0\n\tbuffer_load_dword %1, %2, %3 offen lds" ::"s"(lds_dst), "v"(voff), "s"(srd), "s"(soff) : "memory");
}
__device__ __forceinline__ void sld8(unsigned long long& sink, unsigned long long base, unsigned off) {
  asm volatile("s_load_dwordx2 %0, %1, %2" : "+s"(sink) : "s"(base), "s"(off) : "memory");
}
__device__ __forceinline__ bf16x8 tr_frag(LAS char* p0, LAS char* p1) {
  const bf16x4 lo = __builtin_amdgcn_ds_read_tr16_b64_v4bf16((LAS bf16x4*)p0);
  const bf16x4 hi = __builtin_amdgcn_ds_read_tr16_b64_v4bf16((LAS bf16x4*)p1);
  return __builtin_shufflevector(lo, hi, 0, 1, 2, 3, 4, 5, 6, 7);
}
template <bool WT> __device__ __forceinline__ void gst16(void* p, const u32x4& v) {
  if (WT) asm volatile("global_store_dwordx4 %0, %1, off sc1\n\ts_nop 1" ::"v"(p), "v"(v) : "memory");
  else *(u32x4*)p = v;
}
template <bool WT> __device__ __forceinline__ void gst8(void* p, const u32x2& v) {
  if (WT) asm volatile("global_store_dwordx2 %0, %1, off sc1\n\ts_nop 1" ::"v"(p), "v"(v) : "memory");
  else *(u32x2*)p = v;
}
#define MFMA(a, b, c) __builtin_amdgcn_mfma_f32_16x16x32_bf16(a, b, c, 0, 0, 0)
__device__ __forceinline__ void mfma_acc(f32x4& c, const bf16x8& a, const bf16x8& b) {
  asm volatile("v_mfma_f32_16x16x32_bf16 %0, %1, %2, %0" : "+a"(c) : "v"(a), "v"(b));
}
__device__ __forceinline__ void mfma_acc_va(f32x4& c, const bf16x8& a, const bf16x8& b) {
  asm volatile("v_mfma_f32_16x16x32_bf16 %0, %1, %2, %0" : "+a"(c) : "v"(a), "a"(b));
}
__device__ __forceinline__ void mfma_acc_av(f32x4& c, const bf16x8& a, const bf16x8& b) {
  asm volatile("v_mfma_f32_16x16x32_bf16 %0, %1, %2, %0" : "+a"(c) : "a"(a), "v"(b));
}
template <bool AG0, bool AG1, bool Z>
__device__ __forceinline__ void mfma_x(f32x4& c, const bf16x8& a, const bf16x8& b) {
  if constexpr (!Z) {
    if constexpr (AG0) mfma_acc_av(c, a, b); else if constexpr (AG1) mfma_acc_va(c, a, b); else mfma_acc(c, a, b);
  } else if constexpr (AG0) asm volatile("v_mfma_f32_16x16x32_bf16 %0, %1, %2, 0" : "=&a"(c) : "a"(a), "v"(b));
  else if constexpr (AG1) asm volatile("v_mfma_f32_16x16x32_bf16 %0, %1, %2, 0" : "=&a"(c) : "v"(a), "a"(b));
  else asm volatile("v_mfma_f32_16x16x32_bf16 %0, %1, %2, 0" : "=&a"(c) : "v"(a), "v"(b));
}
__device__ __forceinline__ void gld16a(bf16x8& dst, const u32x4& srd, unsigned voff, unsigned soff) {
  asm volatile("buffer_load_dwordx4 %0, %1, %2, %3 offen" : "=a"(dst) : "v"(voff), "s"(srd), "s"(soff) : "memory");
}
template <bool AG> __device__ __forceinline__ void PINR(bf16x8& x) {
  if (AG) asm volatile("" : "+a"(x)); else asm volatile("" : "+v"(x));
}
__device__ __forceinline__ void PINA(f32x4& c) { asm volatile("" : "+a"(c)); }

template <int MF, int KS, int BI, int ST, int AD, int XR, int PRO, int XE, bool LW>
struct Sched {
  static constexpr int NS = 4 * AD, BP = (BI + KS - 1) / KS, MAXX = 64;
  int total[NS + 1] = {};
  int posA[NS + AD + 1] = {}, posB[NS + ST + 2] = {};
  int nx[AD] = {};
  int xkind[AD][MAXX] = {}, xidx[AD][MAXX] = {}, xpc[AD][MAXX] = {};
  int npro = 0, pkind[MAXX] = {}, pidx[MAXX] = {}, ppc[MAXX] = {};
  constexpr Sched() {
    int n = 0;
    int qk[512] = {}, qi[512] = {}, qp[512] = {}, nq = 0, hq = 0;
    for (int p = 0; p < BI; ++p) { qk[nq] = 1; qi[nq] = 0; qp[nq] = p; ++nq; }
    for (int a = 0; a < AD || (a + 1) / KS < ST; ++a) {
      if (!LW && (a + 1) % KS == 0 && (a + 1) / KS < ST) for (int p = 0; p < BI; ++p) { qk[nq] = 1; qi[nq] = (a + 1) / KS; qp[nq] = p; ++nq; }
      if (a < AD) for (int m = 0; m < MF; ++m) { qk[nq] = 0; qi[nq] = a; qp[nq] = m; ++nq; }
    }
    while (hq < nq && hq < PRO) {
      pkind[hq] = qk[hq]; pidx[hq] = qi[hq]; ppc[hq] = qp[hq];
      ++n; if (qk[hq]) posB[qi[hq]] = n; else posA[qi[hq]] = n;
      ++hq;
    }
    npro = hq;
    for (int j = 0; j < NS; ++j) {
      total[j] = n;
      if (j < AD) {
        int c = 0;
        while (hq < nq && (c < XR || j == AD - 1)) {
          xkind[j][c] = qk[hq]; xidx[j][c] = qi[hq]; xpc[j][c] = qp[hq];
          ++n; if (qk[hq]) posB[qi[hq]] = n; else posA[qi[hq]] = n;
          ++hq; ++c;
        }
        nx[j] = c;
      }
      if (XE && j >= AD && j % XE == 0) ++n;
      if (j >= 1) { n += MF; posA[j - 1 + AD] = n; }
      if (!LW && j + 1 >= KS) {
        const int g = (j + 1) % KS, T = (j + 1) / KS - 1 + ST;
        for (int p = g * BP; p < (g + 1) * BP && p < BI; ++p) { ++n; posB[T] = n; }
      }
    }
    total[NS] = n;
  }
  constexpr int clampv(int v) const { return v > 63 ? 63 : v; }
  constexpr int waitA(int j) const { return clampv(total[j] - posA[j]); }
  constexpr int waitB(int j) const { return LW ? 63 : clampv(total[j] - posB[j / KS + 1]); }
  constexpr int min3(int a, int b, int c) const { return a < b ? (a < c ? a : c) : (b < c ? b : c); }
  constexpr int loopA(int r) const { return min3(waitA(AD + r), waitA(2 * AD + r), waitA(3 * AD + r)); }
  constexpr int loopB(int r) const { return min3(waitB(AD + r), waitB(2 * AD + r), waitB(3 * AD + r)); }
  constexpr bool ok() const {
    for (int j = 0; j < NS; ++j) {
      if (posA[j] <= 0 || posA[j] > total[j]) return false;
      if (!LW && j % KS == KS - 1 && (posB[j / KS + 1] <= 0 || posB[j / KS + 1] > total[j])) return false;
    }
    for (int j = 0; j < AD; ++j) if (nx[j] > MAXX) return false;
    if (posB[0] > total[0] || PRO > MAXX || 3 * AD / KS + 1 < ST) return false;
    return true;
  }
};

template <int MODE_, int MF_, int NF_, int KS_, int ST_, int AD_, int XR_, int PRO_, int KTOT_, int KLD_, int XE_, int MU_ = 0, int LW_ = 0, int SPF_ = 0>
struct Cfg {
  static constexpr int MODE = MODE_, MF = MF_, NF = NF_, KS = KS_, ST = ST_, AD = AD_, XR = XR_, PRO = PRO_, KTOT = KTOT_, KLD = KLD_, XE = XE_, NFX = NF_ + MU_;
  static constexpr int LW = LW_;
  static constexpr int SPF = SPF_, NT = SPF_ ? 512 : (LW_ ? 320 : 256);
  static constexpr int BK = 32 * KS;
  static constexpr int ROWB = (MODE == 3) ? 224 : NF * 32;
  static constexpr int SUB = (MODE == 3) ? BK * ROWB : 0;
  static constexpr int STAGE = BK * NF * 32;
  static constexpr int BI = STAGE / 4096;
  static constexpr int LDS_BYTES = ST * STAGE;
  static_assert((XE == 0 || AD % XE == 0) && STAGE % 4096 == 0 && AD % KS == 0 && AD % 2 == 0 && (KTOT / 32) % AD == 0, "");
};

template <class C> struct SchedOf {
  using SC = Sched<C::MF, C::KS, C::BI, C::ST, C::AD, C::XR, C::PRO, C::XE, (C::LW > 0)>;
  static constexpr SC S{};
};
template <class C> __device__ __forceinline__ int b_swz(int row) {
  if (C::MODE == 3) return 0;
  if (C::MODE == 1) return ((row >> 1) & 1) | (((row >> 3) & 1) << 1);
  if (C::ROWB % 256 == 0) return row & 7;
  return (row >> 1) & 3;
}

struct NoExt { template <class... T> __device__ __forceinline__ void operator()(T&&...) const {} };
template <class C, int NEPI, class Mid, class Ext>
__device__ __forceinline__ bool gemm_main(f32x4 (&acc)[C::MF][C::NFX], LAS char* lds, const u16* A, const u16* B0, const u16* B1, unsigned b_bytes,
                                          int ldb, const u32x4& swarm, const u32x4& spf, Mid&& mid, Ext&& ext TSARG, int cswap = 0) {
  constexpr int MF = C::MF, NF = C::NFX, KS = C::KS, ST = C::ST, AD = C::AD, BI = C::BI, ROWB = C::ROWB, NMM = MF * NF;
  using SO = SchedOf<C>;
  static_assert(SO::S.ok(), "schedule");
  constexpr int BP = SO::SC::BP;
  TSTAMP(0);
  const int lane = threadIdx.x & 63;
  const int wave = __builtin_amdgcn_readfirstlane(threadIdx.x >> 6);
  const unsigned lds0 = (unsigned)(size_t)lds;
  const u32x4 sa = mk_srd(A + (size_t)wave * MF * 16 * C::KLD, (unsigned)((MF - 1) * 16 * C::KLD + 16 * C::KTOT) * 2);
  const u32x4 sb = mk_srd((C::MODE == 3 && wave >= 2) ? B1 : B0, b_bytes);
  unsigned a_off[MF];
#pragma unroll
  for (int m = 0; m < MF; ++m) a_off[m] = lane * 16 + m * (C::KLD * 32);
  const unsigned b_tile = (unsigned)(C::BK * ldb * 2);
  auto src_of = [&](int piece) -> unsigned {
    if (C::MODE == 3) {
      const int ch = (piece % (2 * C::BI)) * 64 + lane, row = ch / 14, c = ch % 14;
      return (unsigned)(row * ldb * 2 + c * 16);
    }
    constexpr int CPR = ROWB / 16;
    const int ch = piece * 64 + lane, row = ch / CPR, cs = ch % CPR, c = cs ^ (b_swz<C>(row) << 1);
    const int col = (C::MODE == 1) ? (c >> 3) * 1024 + (c & 7) * 8 : (c ^ cswap) * 8;
    return (unsigned)(row * ldb * 2 + col * 2);
  };
  if constexpr (C::SPF > 0) {
    if (wave > 4) {
      const unsigned long long base = ((unsigned long long)spf[1] << 32) | spf[0];
      unsigned long long sink = 0;
      unsigned off = (wave - 5) * 128 + 60;
      const unsigned last = spf[2] - 68;
      auto burst = [&](auto NC) {
#pragma unroll
        for (int i = 0; i < decltype(NC)::value; ++i) sld8(sink, base, off + i * 384 < last ? off + i * 384 : last);
        off += decltype(NC)::value * 384;
      };
      burst(std::integral_constant<int, SPB>{});
      BAR();
#pragma unroll 1
      for (int t = 0; t < C::KTOT / C::BK; ++t) {
        if (off <= last) burst(std::integral_constant<int, C::SPF>{});
        BAR();
      }
      if (off <= last) burst(std::integral_constant<int, SPE>{});
      sfor<NEPI>([&](auto) { BAR(); });
      asm volatile("s_waitcnt lgkmcnt(0)" : "+s"(sink)::"memory");
      return false;
    }
  }
  if constexpr (C::LW > 0) {
    if (wave == 4) {
      constexpr int NP = 4 * C::BI, INF = C::LW;
      static_assert(INF >= 2 && INF < ST && INF * NP <= 63, "");
      unsigned src[NP];
#pragma unroll
      for (int i = 0; i < NP; ++i) src[i] = src_of(i);
      auto tile = [&](int slot, unsigned t) {
        sfor<NP>([&](auto i) { glds16(sb, lds0 + slot * C::STAGE + decltype(i)::value * 1024, src[decltype(i)::value], t * b_tile); });
      };
      tile(1, 1);
      BAR();
      sfor<INF - 2>([&](auto t) { tile(decltype(t)::value + 2, decltype(t)::value + 2); });
      int slot = INF;
      unsigned long long sink = 0;
#pragma unroll 1
      for (int t = 0; t < C::KTOT / C::BK; ++t) {
        tile(slot, t + INF);
        slot = (slot + 1 == ST) ? 0 : slot + 1;
        if constexpr (C::SPF > 0) {
          constexpr unsigned o0 = 3 * (SPB + C::KTOT / C::BK * C::SPF + SPE) * 128 + 60;
#pragma unroll
          for (int i = 0; i < SPL; ++i) {
            const unsigned o = o0 + (t * SPL + i) * 128;
            if (o + 8 <= spf[2]) sld8(sink, ((unsigned long long)spf[1] << 32) | spf[0], o);
          }
        }
        WAIT_V((INF - 1) * NP);
        BAR();
      }
      WAIT_V(0);
      sfor<NEPI>([&](auto) { BAR(); });
      asm volatile("s_waitcnt lgkmcnt(0)" : "+s"(sink)::"memory");
      return false;
    }
  }
  unsigned b_src[BI];
#pragma unroll
  for (int i = 0; i < BI; ++i) b_src[i] = src_of(wave * BI + i);
  const unsigned b_dst = lds0 + wave * BI * 1024;

  const int G = lane >> 4, kr = (lane & 15) >> 2, p = lane & 3;
  const int r0 = (C::MODE == 1 ? 8 : 4) * G + kr;
  constexpr int RD2 = (C::MODE == 1 ? 4 : 16) * ROWB;
  constexpr int NV = (C::MODE == 3) ? 1 : (ROWB % 256 == 0 ? 8 : 4);
  int b_rd[NV];
#pragma unroll
  for (int v = 0; v < NV; ++v) b_rd[v] = r0 * ROWB + ((v ^ b_swz<C>(r0)) << 5) + p * 8;
  LAS char* xfr = lds + C::LDS_BYTES + (lane & 1) * 64 + G * 16;
  auto half = [&](LAS char* base, int n, int s, int hf, int step) -> bf16x4 {
    if (n == C::NF) return *(const LAS bf16x4*)(xfr + step * 128 + hf * 8);
    int off;
    if (C::MODE == 3) off = b_rd[0] + (n & 1) * C::SUB + (n >> 1) * 32;
    else off = b_rd[n % NV] + (n / NV) * (NV * 32);
    off += s * 32 * ROWB + hf * RD2;
    return __builtin_amdgcn_ds_read_tr16_b64_v4bf16((LAS bf16x4*)(base + off));
  };

  bf16x8 ar[AD][MF];
  bf16x4 bf[2][NF][2];
  auto issueA = [&](auto RC, auto MC, unsigned step) {
    const unsigned so = step < C::KTOT / 32 ? step * 1024 : 0x7ff00000u;
    if constexpr (C::LW > 0) gld16a(ar[decltype(RC)::value][decltype(MC)::value], sa, a_off[decltype(MC)::value], so);
    else gld16<0>(ar[decltype(RC)::value][decltype(MC)::value], sa, a_off[decltype(MC)::value], so);
  };
  if constexpr (C::NFX > C::NF) glds16(swarm, lds0 + C::LDS_BYTES + wave * 1024, lane * 16, wave * 1024);
  sfor<SO::S.npro>([&](auto LC) {
    constexpr int l = decltype(LC)::value, kind = SO::S.pkind[l], idx = SO::S.pidx[l], pc = SO::S.ppc[l];
    if constexpr (kind) glds16(sb, b_dst + idx * C::STAGE + pc * 1024, b_src[pc], idx * b_tile);
    else issueA(std::integral_constant<int, idx>{}, std::integral_constant<int, pc>{}, idx);
  });
#pragma unroll
  for (int m = 0; m < MF; ++m)
#pragma unroll
    for (int n = 0; n < NF; ++n) if (!ZC) { acc[m][n] = f32x4{0.f, 0.f, 0.f, 0.f}; PINA(acc[m][n]); }
  TSTAMP(1);
  { constexpr int w0 = SO::S.total[0] - SO::S.posB[0]; WAIT_V(w0); }
  BAR();
  TSTAMP(2);
#pragma unroll
  for (int n = 0; n < NF; ++n) { bf[0][n][0] = half(lds, n, 0, 0, 0); bf[0][n][1] = half(lds, n, 0, 1, 0); }
  SB();

  int slot = 0, fslot = 0;
  auto step = [&](auto JC, auto FC, int j0) {
    constexpr int R = decltype(JC)::value;
    constexpr bool FIRST = decltype(FC)::value;
    constexpr int s = R % KS, cur = R & 1, nxt = cur ^ 1;
    constexpr int WA = FIRST ? SO::S.waitA(R) : SO::S.loopA(R), WB = (R % KS != KS - 1) ? 63 : (FIRST ? SO::S.waitB(R) : SO::S.loopB(R));
    WAIT_V(WA);
    sfor<MF>([&](auto m) { PINR<(C::LW > 0)>(ar[R][decltype(m)::value]); });
    if constexpr (s == KS - 1) {
      WAIT_L0();
      if constexpr (WB < WA) WAIT_V(WB);
      BAR();
      fslot = slot;
      slot = (slot + 1 == ST) ? 0 : slot + 1;
    }
    LAS char* rbase = lds + slot * C::STAGE;
    constexpr int ns = (s + 1) % KS;
    constexpr bool HASB = !(FIRST && R + 1 < KS) && C::LW == 0;
    constexpr int g = (R + 1) % KS;
    constexpr int NB = HASB ? ((g + 1) * BP <= BI ? BP : (BI - g * BP > 0 ? BI - g * BP : 0)) : 0;
    constexpr int NX = FIRST ? SO::S.nx[R] : (C::XE && R % (C::XE ? C::XE : 1) == 0 ? 1 : 0);
    constexpr int NA = (FIRST && R == 0) ? 0 : MF, RP = (R + AD - 1) % AD;
    constexpr int NL = NX + NA + NB;
    const unsigned gdst = b_dst + fslot * C::STAGE;
    const unsigned gsoff = (unsigned)((j0 + R + 1) / KS - 1 + ST) * b_tile;
    SB();
    sfor<NMM>([&](auto IC) {
      constexpr int i = decltype(IC)::value, n = i / MF, m = i % MF;
      const bf16x8 bb = __builtin_shufflevector(bf[cur][n][0], bf[cur][n][1], 0, 1, 2, 3, 4, 5, 6, 7);
      constexpr bool Z = ZC && FIRST && R == 0;
      if constexpr (C::MODE == 1 && n >= 4) mfma_x<(C::LW > 0), false, Z>(acc[m][n], ar[R][m], bb);
      else mfma_x<false, (C::LW > 0), Z>(acc[m][n], bb, ar[R][m]);
      SB();
      constexpr int SPAN = (NMM * 7) / 8 >= 2 * NF ? (NMM * 7) / 8 : NMM;
      sfor<2 * NF>([&](auto NC) {
        constexpr int nn = decltype(NC)::value;
        if constexpr ((nn * SPAN) / (2 * NF) == i) bf[nxt][nn >> 1][nn & 1] = half(rbase, nn >> 1, ns, nn & 1, j0 + R + 1);
      });
      sfor<NL>([&](auto LC) {
        constexpr int l = decltype(LC)::value;
        if constexpr (((2 * l + 1) * NMM) / (2 * (NL ? NL : 1)) == i) {
          if constexpr (l < NX && !FIRST) {
            constexpr int XS = C::MODE == 1 ? XS1 : XS3, XT = C::MODE == 1 ? XT1 : XT3;
            glds4(swarm, lds0 + C::LDS_BYTES + wave * 256, lane * XS, j0 + R >= XT ? (unsigned)(j0 + R - XT) * (64 * XS / (C::XE ? C::XE : 1)) : 0x7ff00000u);
          } else if constexpr (l < NX) {
            constexpr int kind = SO::S.xkind[R][l], idx = SO::S.xidx[R][l], pc = SO::S.xpc[R][l];
            if constexpr (kind) glds16(sb, b_dst + idx * C::STAGE + pc * 1024, b_src[pc], idx * b_tile);
            else issueA(std::integral_constant<int, idx>{}, std::integral_constant<int, pc>{}, idx);
          } else if constexpr (l < NX + NA) {
            issueA(std::integral_constant<int, RP>{}, std::integral_constant<int, l - NX>{}, (unsigned)(j0 + R - 1 + AD));
          } else {
            constexpr int pc = g * BP + (l - NX - NA);
            glds16(sb, gdst + pc * 1024, b_src[pc], gsoff);
          }
        }
      });
      SB();
    });
  };
  sfor<AD>([&](auto r) { step(r, std::true_type{}, 0); });
  mid();
  SB();
#pragma unroll 1
  for (int j0 = AD; j0 < C::KTOT / 32; j0 += AD) sfor<AD>([&](auto r) { step(r, std::false_type{}, j0); });
  asm volatile("s_nop 15\n\ts_nop 15" ::: "memory");
#pragma unroll
  for (int m = 0; m < MF; ++m)
#pragma unroll
    for (int n = 0; n < NF; ++n) PINA(acc[m][n]);
  TSTAMP(3);
  ext();
  WAIT_V(0);
  sfor<AD>([&](auto r) { sfor<MF>([&](auto m) { PINR<(C::LW > 0)>(ar[decltype(r)::value][decltype(m)::value]); }); });
  WAIT_L0();
  TSTAMP(5);
  return true;
}
#define C1_SPF 6
#define C1_ST 4
#define C1_AD 4
#define C1_XR 6
#define C2_ST 4
#define C2_XR 3
#define C3_ST 4
#define C3_XR 10
#define C4_ST 4
#define C4_XR 4
#define C1_PRO 16
#define C2_PRO 16
#define C3_PRO 18
#define C4_PRO 16
#define MAP4 0
#define WTALL 1
constexpr bool WT0 = WTALL, WT1 = WTALL, WTO = WTALL, WT2 = WTALL, WT3 = true, WT4 = WTALL;
#define PFMODE 2
#define C1_XE (C1_SPF ? 0 : 4)
#define C3_XE 4
#define C1_LW 2
#define C2_LW 3
#define C4_LW 3
#define EPQ 2
#define PFR 704
#define C2_SPF 0
#define PFRO PFR
#define PFR2 PFRO
static_assert(PFR % 16 == 0 && PFRO % 16 == 0 && PFR2 % 16 == 0 && (C2_SPF == 0 || PFR2 > PFRO) && PFR <= PFRO && PFRO <= PFR2 && PFR2 <= 1024 && (PFRO - PFR) * 14 <= 8192 && (1024 - PFR2) * 7 <= 4096, "");
#define LNR 1024
#define ALIAS 2
#define CKD 1
#define COWN 1
#define L1SYM "_Z10ln1_kernelPKtPtS0_S0_PfS1_Pj"
#define G1SYM "_Z12gemm1_kernelPKtS0_PtPfS0_S0_"

__device__ __forceinline__ float phi(float t) {
  const float e = __builtin_amdgcn_fmed3f(__builtin_amdgcn_exp2f(t * 1.4426950408889634f), 0.f, 1.f);
  return (fmaxf(t, 0.f) + e) + 1e-6f;
}
__device__ __forceinline__ bf16x8 pack8(const f32x4& a, const f32x4& b) {
  u32x4 o = {pack_bf16(a[0], a[1]), pack_bf16(a[2], a[3]), pack_bf16(b[0], b[1]), pack_bf16(b[2], b[3])};
  return __builtin_bit_cast(bf16x8, o);
}
__device__ __forceinline__ f32x4 phi4(const f32x4& a) { return f32x4{phi(a[0]), phi(a[1]), phi(a[2]), phi(a[3])}; }

constexpr int KVP = 1024 + 32;
using C1 = Cfg<1, 2, 12, 2, C1_ST, C1_AD, C1_XR, C1_PRO, 1024, 1024, C1_XE, 0, C1_LW, C1_SPF>;
constexpr int LDS1 = C1::LDS_BYTES + 1024 + 48 * 1024;
__global__ __launch_bounds__(C1::NT, 1) void gemm1_kernel(const u16* __restrict__ xn, const u16* __restrict__ wqkv, u16* __restrict__ pq,
                                                       float* __restrict__ kvp, const u16* __restrict__ wg,
                                                       const u16* __restrict__ wu) {
  extern __shared__ __attribute__((aligned(16))) char smem[];
  LAS char* lds = (LAS char*)smem;
  const int b = blockIdx.x, xcd = b & 7, idx = b >> 3;
  const int tm = (xcd >> 2) * 8 + (idx & 7), tn = (xcd & 3) * 4 + (idx >> 3);
  f32x4 acc[2][12];
  TSDECL;
  const int lane = threadIdx.x & 63, wave = __builtin_amdgcn_readfirstlane(threadIdx.x >> 6), gw = b * 4 + wave;
  const u32x4 sw = mk_srd(b < 128 ? wg + b * (PFR * 28) : wu + (b - 128) * (PFR * 28), PFR * 56);
  if (!gemm_main<C1, 1>(acc, lds, xn + (size_t)tm * 128 * D_, wqkv + tn * 64, wqkv, (unsigned)(D_ * 3 * D_ - tn * 64) * 2, 3 * D_, sw, sw, [] {}, NoExt{} TSPASS)) return;
  LAS f32x4* red = (LAS f32x4*)(lds + C1::LDS_BYTES + 1024);
  const u32x4 one4 = {0x3f803f80u, 0x3f803f80u, 0x3f803f80u, 0x3f803f80u};
  const f32x4 z4 = {0.f, 0.f, 0.f, 0.f};
#pragma unroll
  for (int hh = 0; hh < 2; ++hh) {
    const bf16x8 v0 = pack8(acc[0][8 + 2 * hh], acc[1][8 + 2 * hh]), v1 = pack8(acc[0][9 + 2 * hh], acc[1][9 + 2 * hh]);
#pragma unroll
    for (int i = 0; i < 2; ++i) {
      const bf16x8 pk = pack8(phi4(acc[0][4 + 2 * hh + i]), phi4(acc[1][4 + 2 * hh + i]));
      red[((wave * 2 + hh) * 6 + 2 * i) * 64 + lane] = MFMA(pk, v0, z4);
      red[((wave * 2 + hh) * 6 + 2 * i + 1) * 64 + lane] = MFMA(pk, v1, z4);
      red[((wave * 2 + hh) * 6 + 4 + i) * 64 + lane] = MFMA(pk, __builtin_bit_cast(bf16x8, one4), z4);
    }
  }
  SB();
#pragma unroll
  for (int m = 0; m < 2; ++m)
#pragma unroll
    for (int hh = 0; hh < EPQ; ++hh)
      gst16<WT1>(pq + ((size_t)((tm * 8 + wave * 2 + m) * 32 + tn * 2 + hh) * 64 + lane) * 8, __builtin_bit_cast(u32x4, pack8(phi4(acc[m][2 * hh]), phi4(acc[m][2 * hh + 1]))));
  WAIT_L0();
  BAR();
#pragma unroll
  for (int e = 0; e < 3; ++e) {
    const int blk = e * 4 + wave, hh = blk / 6, bb = blk % 6;
    const f32x4 s = (red[blk * 64 + lane] + red[(12 + blk) * 64 + lane]) + (red[(24 + blk) * 64 + lane] + red[(36 + blk) * 64 + lane]);
    float* dst = kvp + ((size_t)(tn * 2 + hh) * 16 + tm) * KVP;
    if (bb < 4) gst16<WT1>(dst + bb * 256 + lane * 4, __builtin_bit_cast(u32x4, s));
    else if ((lane & 15) == 0) gst16<WT1>(dst + 1024 + (bb - 4) * 16 + (lane >> 4) * 4, __builtin_bit_cast(u32x4, s));
  }
  SB();
#pragma unroll
  for (int m = 0; m < 2; ++m)
#pragma unroll
    for (int hh = EPQ; hh < 2; ++hh)
      gst16<WT1>(pq + ((size_t)((tm * 8 + wave * 2 + m) * 32 + tn * 2 + hh) * 64 + lane) * 8, __builtin_bit_cast(u32x4, pack8(phi4(acc[m][2 * hh]), phi4(acc[m][2 * hh + 1]))));
  WAIT_V(0);
  TSTAMP(4);
  TSOUT(0, 6);
}

__global__ __launch_bounds__(256) void o_kernel(const u16* __restrict__ pq, const float* __restrict__ kvp, u16* __restrict__ o,
                                                const u16* __restrict__ wg, const u16* __restrict__ wu) {
  __shared__ __attribute__((aligned(16))) float sh[1024];
  const int t = threadIdx.x, lane = t & 63, wave = __builtin_amdgcn_readfirstlane(t >> 6);
  const int head = blockIdx.x & 31, rg = blockIdx.x >> 5;
  const u32x4 sk = mk_srd(kvp + (size_t)head * 16 * KVP, 16 * KVP * 4), sq = mk_srd(pq + ((size_t)(rg * 16 + wave * 4) * 32 + head) * 512, 100 * 1024);
  const int gw = blockIdx.x * 4 + wave;
  const u32x4 sw = mk_srd((gw < 512 ? wg : wu) + (size_t)PFR * F_ + (gw & 511) * ((PFRO - PFR) * 7), (PFRO - PFR) * 14);
  f32x4 pv[16], zp;
  bf16x8 qf[4];
  float pf[2];
  sfor<16>([&](auto i) { gld16<0>(pv[decltype(i)::value], sk, t * 16, decltype(i)::value * KVP * 4); });
  gld16<0>(zp, sk, t < 128 ? (t >> 3) * KVP * 4 + 4096 + (t & 7) * 16 : 0x7ffffff0u, 0);
  sfor<4>([&](auto m) { gld16<0>(qf[decltype(m)::value], sq, lane * 16, decltype(m)::value * 32768); });
  gld4(pf[0], sw, lane * 64, 0);
  gld4(pf[1], sw, lane * 64, 4096);
  WAIT_V(2);
  sfor<16>([&](auto i) { PIN(pv[decltype(i)::value]); });
  sfor<4>([&](auto m) { PIN(qf[decltype(m)::value]); });
  PIN(zp);
#pragma unroll
  for (int st = 8; st > 0; st >>= 1)
#pragma unroll
    for (int i = 0; i < st; ++i) pv[i] += pv[i + st];
  u32x2 pk2 = {pack_bf16(pv[0][0], pv[0][1]), pack_bf16(pv[0][2], pv[0][3])};
  *(u32x2*)((char*)sh + ((wave & 1) * 64 + lane) * 16 + (wave >> 1) * 8) = pk2;
  if (t < 128) *(f32x4*)(sh + 512 + t * 4) = zp;
  __syncthreads();
  const bf16x8 kv0 = *(const bf16x8*)((char*)sh + lane * 16), kv1 = *(const bf16x8*)((char*)sh + (64 + lane) * 16);
  f32x4 za = {0.f, 0.f, 0.f, 0.f}, zb = za;
#pragma unroll
  for (int i = 0; i < 16; ++i) {
    za += *(const f32x4*)(sh + 512 + i * 32 + (lane >> 4) * 4);
    zb += *(const f32x4*)(sh + 512 + i * 32 + 16 + (lane >> 4) * 4);
  }
  const bf16x8 zf = pack8(za, zb);
  const f32x4 z4 = {0.f, 0.f, 0.f, 0.f};
#pragma unroll
  for (int m = 0; m < 4; ++m) {
    const f32x4 n0 = MFMA(kv0, qf[m], z4), n1 = MFMA(kv1, qf[m], z4), dn = MFMA(zf, qf[m], z4);
    const float inv = 1.f / (dn[0] + 1e-6f);
    gst16<WTO>(o + ((size_t)((rg * 16 + wave * 4 + m) * 32 + head) * 64 + lane) * 8, __builtin_bit_cast(u32x4, pack8(n0 * inv, n1 * inv)));
  }
  WAIT_V(0);
  PIN(pf[0]); PIN(pf[1]);
}

using C2 = Cfg<2, 2, 4, 4, C2_ST, 8, C2_XR, C2_PRO, 1024, 1024, 0, 1, C2_LW, C2_SPF>;
constexpr int LDS2 = C2::LDS_BYTES + 4096 + 256;
__global__ __launch_bounds__(C2::NT, 1) void gemm2_kernel(const u16* __restrict__ o, const u16* __restrict__ wo, const u16* __restrict__ x,
                                                       u16* __restrict__ h, float* __restrict__ stats, const float* __restrict__ sumx,
                                                       const u16* __restrict__ rsw, float* __restrict__ mub,
                                                       const u16* __restrict__ wg, const u16* __restrict__ wu) {
  extern __shared__ __attribute__((aligned(16))) char smem[];
  LAS char* lds = (LAS char*)smem;
  const int b = blockIdx.x, xcd = b & 7, idx = b >> 3;
  const int tm = (xcd >> 1) * 4 + (idx & 3), tn = (xcd & 1) * 8 + (idx >> 2);
  const int lane = threadIdx.x & 63, wave = __builtin_amdgcn_readfirstlane(threadIdx.x >> 6);
  f32x4 acc[2][5];
  u32x2 res[2][4];
  float sxr[2];
  const u32x4 sx = mk_srd(x + (size_t)(tm * 128 + wave * 32) * D_ + tn * 64, 32 * D_ * 2);
  const u32x4 ssx = mk_srd(sumx + tm * 128 + wave * 32, 128), sr = mk_srd(rsw, 4096);
  const unsigned xo = (unsigned)((lane & 15) * D_ * 2 + (lane >> 4) * 8);
  const u32x4 sp = mk_srd((b < 128 ? wg : wu) + (size_t)PFRO * F_ + (b & 127) * ((PFR2 - PFRO) * 28), (PFR2 - PFRO) * 56);
  TSDECL;
  if (!gemm_main<C2, 0>(acc, lds, o + (size_t)tm * 128 * D_, wo + tn * 64, wo, (unsigned)(D_ * D_ - tn * 64) * 2, D_, sr, sp, [&] {
    sfor<2>([&](auto m) {
      sfor<4>([&](auto n) { gld8<decltype(n)::value * 32>(res[decltype(m)::value][decltype(n)::value], sx, xo, decltype(m)::value * 16 * D_ * 2); });
      gld4(sxr[decltype(m)::value], ssx, (lane & 15) * 4, decltype(m)::value * 64);
    });
  }, NoExt{} TSPASS)) return;
#pragma unroll
  for (int m = 0; m < 2; ++m) {
    unsigned w[8];
    PIN(sxr[m]);
    const float mu = (sxr[m] + (acc[m][4][0] + acc[m][4][1])) * (1.f / D_);
#pragma unroll
    for (int n = 0; n < 4; ++n) {
      PIN(res[m][n]);
      w[2 * n] = pack_bf16(bf_lo(res[m][n][0]) + acc[m][n][0] - mu, bf_hi(res[m][n][0]) + acc[m][n][1] - mu);
      w[2 * n + 1] = pack_bf16(bf_lo(res[m][n][1]) + acc[m][n][2] - mu, bf_hi(res[m][n][1]) + acc[m][n][3] - mu);
    }
    const int mb = tm * 8 + wave * 2 + m;
    gst16<WT2>(h + ((size_t)(mb * 32 + tn * 2) * 64 + lane) * 8, u32x4{w[0], w[1], w[2], w[3]});
    gst16<WT2>(h + ((size_t)(mb * 32 + tn * 2 + 1) * 64 + lane) * 8, u32x4{w[4], w[5], w[6], w[7]});
    float q = 0.f;
#pragma unroll
    for (int i = 0; i < 8; ++i) { const float a = bf_lo(w[i]), c = bf_hi(w[i]); q += a * a + c * c; }
    q += __shfl_xor(q, 16); q += __shfl_xor(q, 32);
    if (lane < 16) {
      stats[(size_t)(mb * 16 + lane) * 16 + tn] = q;
      if (tn == 0) mub[mb * 16 + lane] = mu;
    }
  }
  TSTAMP(4);
  TSOUT(1, 6);
}

using C3 = Cfg<3, 4, 14, 2, C3_ST, 4, C3_XR, C3_PRO, 1024, 1024, C3_XE>;
constexpr int LDS3 = C3::LDS_BYTES + 1024;
template <int PAR>
__device__ __forceinline__ void geglu_store(f32x4 (&acc)[4][14], const float (&var)[4], u16* g, int mb0, int tn, int lane) {
#pragma unroll
  for (int m = 0; m < 4; ++m) {
    const float ir2 = var[m], r = __builtin_amdgcn_rsqf(ir2), k1 = -2.f * 0.7978845608028654f * 1.4426950408889634f * r, k2 = k1 * 0.044715f * r * r;
    char* dst = (char*)g + ((size_t)(mb0 + m) * (F_ / 32) + ((tn * 7) >> 1)) * 1024;
    unsigned w[14];
#pragma unroll
    for (int n = 0; n < 7; ++n) {
      const f32x4 a = acc[m][2 * n], u = acc[m][2 * n + 1];
      const f32x4 arg = a * (a * a * k2 + k1);
      f32x4 d;
#pragma unroll
      for (int i = 0; i < 4; ++i) d[i] = __builtin_amdgcn_exp2f(arg[i]);
      d = d * ir2 + ir2;
#pragma unroll
      for (int i = 0; i < 4; ++i) d[i] = __builtin_amdgcn_rcpf(d[i]);
      const f32x4 o = a * u * d;
      w[2 * n] = pack_bf16(o[0], o[1]);
      w[2 * n + 1] = pack_bf16(o[2], o[3]);
    }
    if (PAR == 0) {
#pragma unroll
      for (int i = 0; i < 3; ++i) gst16<WT3>(dst + i * 1024 + lane * 16, u32x4{w[4 * i], w[4 * i + 1], w[4 * i + 2], w[4 * i + 3]});
      gst8<WT3>(dst + 3 * 1024 + lane * 16, u32x2{w[12], w[13]});
    } else {
      gst8<WT3>(dst + lane * 16 + 8, u32x2{w[0], w[1]});
#pragma unroll
      for (int i = 0; i < 3; ++i) gst16<WT3>(dst + (i + 1) * 1024 + lane * 16, u32x4{w[4 * i + 2], w[4 * i + 3], w[4 * i + 4], w[4 * i + 5]});
    }
  }
}
__global__ __launch_bounds__(256, 1) void gemm3_kernel(const u16* __restrict__ h, const u16* __restrict__ wg, const u16* __restrict__ wu,
                                                       const float* __restrict__ stats, u16* __restrict__ g,
                                                       const u16* __restrict__ wd) {
  extern __shared__ __attribute__((aligned(16))) char smem[];
  LAS char* lds = (LAS char*)smem;
  const int b = blockIdx.x, xcd = b & 7, idx = b >> 3;
  const int tm = idx & 7, tn = xcd * 4 + (idx >> 3);
  const int lane = threadIdx.x & 63, wave = __builtin_amdgcn_readfirstlane(threadIdx.x >> 6);
  f32x4 acc[4][14];
  f32x4 st[4];
  const u32x4 ss = mk_srd(stats + (size_t)(tm * 256 + wave * 64) * 16, 64 * 64);
  const u32x4 sw = mk_srd(wd + (b * 4 + wave) * 3584, 7168);
  TSDECL;
  gemm_main<C3, 0>(acc, lds, h + (size_t)tm * 256 * D_, wg + tn * 112, wu + tn * 112, (unsigned)(D_ * F_ - tn * 112) * 2, F_, sw, sw, [&] {
    sfor<4>([&](auto i) { gld16<decltype(i)::value * 16>(st[decltype(i)::value], ss, lane * 64, 0); });
    {
      constexpr int HB = (1024 - PFR2) * 7;
      const int gw = b * 4 + wave;
      const u32x4 s2 = mk_srd((gw < 512 ? wg : wu) + (size_t)PFR2 * F_ + (gw & 511) * (HB / 2), 512 * HB + HB);
      const unsigned vo = lane * 64 < HB ? lane * 64 : 0x7ff00000u;
      glds4(s2, (unsigned)(size_t)lds + C3::LDS_BYTES + wave * 256, vo, 0);
      glds4(s2, (unsigned)(size_t)lds + C3::LDS_BYTES + wave * 256, vo, 512 * HB);
    }
  }, [&] {
  float var[4];
  {
    sfor<4>([&](auto i) { PIN(st[decltype(i)::value]); });
    const f32x4 q4 = (st[0] + st[1]) + (st[2] + st[3]);
    const float v = ((q4[0] + q4[1]) + (q4[2] + q4[3])) * (1.f / D_) + 1e-5f;
#pragma unroll
    for (int m = 0; m < 4; ++m) var[m] = __shfl(v, m * 16 + (lane & 15));
  }
  TSTAMP(6);
  if (tn & 1) geglu_store<1>(acc, var, g, tm * 16 + wave * 4, tn, lane);
  else geglu_store<0>(acc, var, g, tm * 16 + wave * 4, tn, lane);
  } TSPASS);
  TSTAMP(4);
  TSOUT(2, 7);
}

using C4 = Cfg<2, 2, 8, 2, C4_ST, 8, C4_XR, C4_PRO, F_ / 2, F_, 0, 0, C4_LW>;
typedef __attribute__((address_space(1))) unsigned gu32;
__global__ __launch_bounds__(C4_LW ? 320 : 256, 1) void gemm4_kernel(const u16* __restrict__ g, const u16* __restrict__ wd, const u16* __restrict__ h,
                                                       const float* __restrict__ mub, u16* __restrict__ out, float* xbuf, unsigned* flags) {
  extern __shared__ __attribute__((aligned(16))) char smem[];
  LAS char* lds = (LAS char*)smem;
  const int b = blockIdx.x, xcd = b & 7, idx = b >> 3;
  const int kh = xcd & 1, tm = ((xcd >> 1) & 1) * 8 + (idx & 7), tn2 = (xcd >> 2) * 4 + (idx >> 3), tn = tn2 * 2 + kh;
  const int lane = threadIdx.x & 63, wave = __builtin_amdgcn_readfirstlane(threadIdx.x >> 6);
  f32x4 acc[2][8];
  u32x4 res[2][2];
  float mu[2];
  const u32x4 smu = mk_srd(mub + tm * 128 + wave * 32, 128);
  const u32x4 sh = mk_srd(h + ((size_t)(tm * 8 + wave * 2) * 32 + tn * 2) * 512, 34 * 1024);
  TSDECL;
  if (!gemm_main<C4, 1>(acc, lds, g + (size_t)tm * 128 * F_ + kh * (F_ / 2 * 16), wd + (size_t)kh * (F_ / 2) * D_ + tn2 * 128, wd,
                (unsigned)(F_ / 2 * D_ - tn2 * 128) * 2, D_, sh, sh, [&] {
    sfor<2>([&](auto m) {
      gld16<0>(res[decltype(m)::value][0], sh, lane * 16, decltype(m)::value * 32768);
      gld16<1024>(res[decltype(m)::value][1], sh, lane * 16, decltype(m)::value * 32768);
      gld4(mu[decltype(m)::value], smu, (lane & 15) * 4, decltype(m)::value * 64);
    });
  }, NoExt{} TSPASS, kh * 8)) return;
  const u32x4 so = mk_srd(xbuf + (size_t)(b * 4 + wave) * 2048, 8192), si = mk_srd(xbuf + (size_t)((b ^ 1) * 4 + wave) * 2048, 8192);
  sfor<8>([&](auto IC) {
    constexpr int i = decltype(IC)::value;
    xst16<(i & 3) * 1024>(acc[i >> 2][4 + (i & 3)], so, lane * 16, (i >> 2) * 4096);
  });
  WAIT_V(0);
  BAR();
  if (threadIdx.x == 0) __hip_atomic_store((gu32*)(flags + b * 32), 1u, __ATOMIC_RELAXED, __HIP_MEMORY_SCOPE_AGENT);
  TSTAMP(6);
  for (int spin = 0; spin < (1 << 22); ++spin) {
    if (__builtin_amdgcn_readfirstlane(__hip_atomic_load((gu32*)(flags + (b ^ 1) * 32), __ATOMIC_RELAXED, __HIP_MEMORY_SCOPE_AGENT))) break;
    __builtin_amdgcn_s_sleep(1);
  }
  TSTAMP(7);
  f32x4 oth[2][4];
  sfor<8>([&](auto IC) {
    constexpr int i = decltype(IC)::value;
    xld16<(i & 3) * 1024>(oth[i >> 2][i & 3], si, lane * 16, (i >> 2) * 4096);
  });
  LAS char* patch = lds + wave * (32 * 144);
  sfor<8>([&](auto IC) {
    constexpr int i = decltype(IC)::value, m = i >> 2, n = i & 3;
    PIN(res[m][n >> 1]); PIN(mu[m]);
    const unsigned r0 = res[m][n >> 1][(n & 1) * 2], r1 = res[m][n >> 1][(n & 1) * 2 + 1];
    const f32x4 base = f32x4{bf_lo(r0), bf_hi(r0), bf_lo(r1), bf_hi(r1)} + mu[m];
    WAIT_V(7 - i);
    PIN(oth[m][n]);
    const f32x4 v = base + (kh ? oth[m][n] + acc[m][n] : acc[m][n] + oth[m][n]);
    *(LAS u32x2*)(patch + (m * 16 + (lane & 15)) * 144 + n * 32 + (lane >> 4) * 8) = u32x2{pack_bf16(v[0], v[1]), pack_bf16(v[2], v[3])};
  });
#pragma unroll
  for (int i = 0; i < 4; ++i) {
    const int row = i * 8 + (lane >> 3);
    const u32x4 w = *(const LAS u32x4*)(patch + row * 144 + (lane & 7) * 16);
    gst16<WT4>(out + (size_t)(tm * 128 + wave * 32 + row) * D_ + tn * 64 + (lane & 7) * 8, w);
  }
  TSTAMP(4);
  TSOUT(3, 8);
}

__global__ __launch_bounds__(256) void ln1_kernel(const u16* __restrict__ x, u16* __restrict__ y, const u16* __restrict__ wqkv,
                                                  const u16* __restrict__ wo, float* __restrict__ sumx, u16* __restrict__ rsw,
                                                  unsigned* __restrict__ flags) {
  __shared__ float red[2][4][8];
  const int t = threadIdx.x, wave = t >> 6, r = t & 7, c0 = t >> 3, row = blockIdx.x * 8 + r;
  u32x4 v[4], pf[6];
  float cw[2];
  {
    unsigned long long pc;
    int d0, d1, d2, d3;
    asm volatile("s_getpc_b64 %0\n\ts_mov_b32 %1, " G1SYM "@rel32@lo+4\n\ts_mov_b32 %2, .Lln1_tail@rel32@lo+12\n\t"
                 "s_mov_b32 %3, " G1SYM ".kd@rel32@lo+20\n\ts_mov_b32 %4, " L1SYM ".kd@rel32@lo+28"
                 : "=&s"(pc), "=s"(d0), "=s"(d1), "=s"(d2), "=s"(d3));
    const u32x4 sc = mk_srd((const char*)(pc + (long long)d0), d1 > d0 ? (unsigned)(d1 - d0) : 0u);
    const u32x4 sd = mk_srd((const char*)(pc + (long long)d2), CKD && d3 > d2 ? (unsigned)(d3 - d2) : 0u);
    const unsigned own = (unsigned)(-d0) & ~63u;
    gld4(cw[0], sc, wave == 0 && COWN ? own + t * 64 : ((blockIdx.x >> 3) * 192 + t - 64) * 16u, 0);
    gld4(cw[1], sd, t * 64, 0);
  }
  const u32x4 sx = mk_srd(x + (size_t)blockIdx.x * 8 * D_, 8 * D_ * 2);
  sfor<4>([&](auto i) { gld16<decltype(i)::value * 512>(v[decltype(i)::value], sx, r * D_ * 2 + c0 * 16, 0); });
  const u32x4 s1 = mk_srd(wqkv + (size_t)blockIdx.x * (LNR * 12), LNR * 24);
  sfor<6>([&](auto i) { gld16<0>(pf[decltype(i)::value], s1, t * 16, decltype(i)::value * 4096); });
  u32x4 wr[2];
  const u32x4 s2 = mk_srd(wo + (size_t)blockIdx.x * 4096, 8192);
  gld16<0>(wr[0], s2, t * 32, 0);
  gld16<16>(wr[1], s2, t * 32, 0);
  WAIT_V(8);
  sfor<4>([&](auto i) { PIN(v[decltype(i)::value]); });
  float s = 0.f;
#pragma unroll
  for (int i = 0; i < 4; ++i)
#pragma unroll
    for (int j = 0; j < 4; ++j) s += bf_lo(v[i][j]) + bf_hi(v[i][j]);
  s += __shfl_xor(s, 8); s += __shfl_xor(s, 16); s += __shfl_xor(s, 32);
  if ((t & 63) < 8) red[0][wave][r] = s;
  __syncthreads();
  const float sx4 = red[0][0][r] + red[0][1][r] + red[0][2][r] + red[0][3][r];
  const float m = sx4 * (1.f / D_);
  if (t < 8) sumx[row] = sx4;
  if (t == 8) flags[blockIdx.x * 32] = 0u;
  float q = 0.f;
#pragma unroll
  for (int i = 0; i < 4; ++i)
#pragma unroll
    for (int j = 0; j < 4; ++j) { const float a = bf_lo(v[i][j]) - m, b = bf_hi(v[i][j]) - m; q += a * a + b * b; }
  q += __shfl_xor(q, 8); q += __shfl_xor(q, 16); q += __shfl_xor(q, 32);
  if ((t & 63) < 8) red[1][wave][r] = q;
  __syncthreads();
  const float var = (red[1][0][r] + red[1][1][r] + red[1][2][r] + red[1][3][r]) * (1.f / D_);
  const float rs = 1.f / sqrtf(var + 1e-5f);
#pragma unroll
  for (int i = 0; i < 4; ++i) {
    u32x4 o;
#pragma unroll
    for (int j = 0; j < 4; ++j) o[j] = pack_bf16((bf_lo(v[i][j]) - m) * rs, (bf_hi(v[i][j]) - m) * rs);
    const int c = c0 + 32 * i;
    gst16<WT0>(y + ((size_t)((row >> 4) * 32 + (c >> 2)) * 64 + (c & 3) * 16 + (row & 15)) * 8, o);
  }
  WAIT_V(0);
  sfor<6>([&](auto i) { PIN(pf[decltype(i)::value]); });
  PIN(wr[0]); PIN(wr[1]);
  PIN(cw[0]); PIN(cw[1]);
  float ws = 0.f;
#pragma unroll
  for (int j = 0; j < 4; ++j) ws += (bf_lo(wr[0][j]) + bf_hi(wr[0][j])) + (bf_lo(wr[1][j]) + bf_hi(wr[1][j]));
#pragma unroll
  for (int d = 1; d < 64; d <<= 1) ws += __shfl_xor(ws, d);
  if ((t & 63) == 0) {
    const int k = blockIdx.x * 4 + wave, kk = k & 31;
    const unsigned hi = pack_bf16(ws, 0.f), lo = pack_bf16(ws - bf_lo(hi), 0.f);
    u16* dst = rsw + (k >> 5) * 64 + ((kk & 15) >> 2) * 8 + (kk & 3) + (kk >> 4) * 4;
    dst[0] = (u16)hi;
    dst[32] = (u16)lo;
  }
  asm volatile(".Lln1_tail:");
}

extern "C" void kernel_launch(void* const* d_in, const int* in_sizes, int n_in, void* d_out, int out_size, void* d_ws, size_t ws_size,
                              hipStream_t stream) {
  const u16* x = (const u16*)d_in[0];
  const u16* wqkv = (const u16*)d_in[1];
  const u16* wo = (const u16*)d_in[2];
  const u16* wg = (const u16*)d_in[4];
  const u16* wu = (const u16*)d_in[5];
  const u16* wd = (const u16*)d_in[6];
  u16* out = (u16*)d_out;
  char* ws = (char*)d_ws;
  const size_t MB = 1 << 20;
  u16* xn = (u16*)(ws);
  u16* pq = (u16*)(ws + 4 * MB);
  u16* o = (u16*)(ws + 8 * MB);
  u16* hbuf = (u16*)(ws + 16 * MB);
  float* kvp = (float*)(ws + 12 * MB);
  float* stats = (float*)(ws + 20 * MB);
  float* sumx = (float*)(ws + 21 * MB);
  float* mub = (float*)(ws + 21 * MB + 65536);
  u16* rsw = (u16*)(ws + 21 * MB + 131072);
  u16* g = (u16*)(ws + (ALIAS == 2 ? 0 : 24) * MB);
  unsigned* flags = (unsigned*)(ws + 22 * MB);
  float* xbuf = (float*)(ws + (ALIAS == 1 ? 0 : 40) * MB);

  (void)hipFuncSetAttribute((const void*)gemm1_kernel, hipFuncAttributeMaxDynamicSharedMemorySize, LDS1);
  (void)hipFuncSetAttribute((const void*)gemm2_kernel, hipFuncAttributeMaxDynamicSharedMemorySize, LDS2);
  (void)hipFuncSetAttribute((const void*)gemm3_kernel, hipFuncAttributeMaxDynamicSharedMemorySize, LDS3);
  (void)hipFuncSetAttribute((const void*)gemm4_kernel, hipFuncAttributeMaxDynamicSharedMemorySize, C4::LDS_BYTES);

  ln1_kernel<<<256, 256, 0, stream>>>(x, xn, wqkv, wo, sumx, rsw, flags);
  gemm1_kernel<<<256, C1::NT, LDS1, stream>>>(xn, wqkv, pq, kvp, wg, wu);
  o_kernel<<<256, 256, 0, stream>>>(pq, kvp, o, wg, wu);
  gemm2_kernel<<<256, C2::NT, LDS2, stream>>>(o, wo, x, hbuf, stats, sumx, rsw, mub, wg, wu);
  gemm3_kernel<<<256, 256, LDS3, stream>>>(hbuf, wg, wu, stats, g, wd);
  gemm4_kernel<<<256, C4_LW ? 320 : 256, C4::LDS_BYTES, stream>>>(g, wd, hbuf, mub, out, xbuf, flags);
}
```

```cpp
#include <hip/hip_runtime.h>
#include <hip/hip_bf16.h>
#include <stdint.h>
#include <type_traits>
#include <utility>

#define LAS __attribute__((address_space(3)))
typedef unsigned short u16;
typedef __bf16 bf16x4 __attribute__((ext_vector_type(4)));
typedef __bf16 bf16x8 __attribute__((ext_vector_type(8)));
typedef float f32x4 __attribute__((ext_vector_type(4)));
typedef float f32x2 __attribute__((ext_vector_type(2)));
typedef unsigned u32x4 __attribute__((ext_vector_type(4)));
typedef unsigned u32x2 __attribute__((ext_vector_type(2)));
typedef __bf16 bf16x2 __attribute__((ext_vector_type(2)));

#define SPE 0
#define SPL 0
#define SPB 8
#define XS1 64
#define XS3 32
#define XT1 4
#define XT3 12
#define ZC 1
constexpr int S_ = 2048, D_ = 1024, F_ = 3584;

__device__ __forceinline__ unsigned pack_bf16(float lo, float hi) {
  bf16x2 v; v[0] = (__bf16)lo; v[1] = (__bf16)hi;
  return __builtin_bit_cast(unsigned, v);
}
__device__ __forceinline__ float bf_lo(unsigned u) { return __builtin_bit_cast(float, u << 16); }
__device__ __forceinline__ float bf_hi(unsigned u) { return __builtin_bit_cast(float, u & 0xffff0000u); }

#define WAIT_V(n) asm volatile("s_waitcnt vmcnt(%0)" ::"i"(n) : "memory")
#define WAIT_L0() asm volatile("s_waitcnt lgkmcnt(0)" ::: "memory")
#define BAR() asm volatile("s_barrier" ::: "memory")
#define SB() __builtin_amdgcn_sched_barrier(0)
template <class T> __device__ __forceinline__ void PIN(T& x) { asm volatile("" : "+v"(x)); }

#define TSDECL
#define TSARG
#define TSPASS
#define TSTAMP(s)
#define TSOUT(k, n)

template <int... I, class Fn>
__device__ __forceinline__ void sfor_impl(std::integer_sequence<int, I...>, Fn&& f) { (f(std::integral_constant<int, I>{}), ...); }
template <int N, class Fn>
__device__ __forceinline__ void sfor(Fn&& f) { sfor_impl(std::make_integer_sequence<int, N>{}, f); }

__device__ __forceinline__ u32x4 mk_srd(const void* p, unsigned bytes) {
  const unsigned long long a = (unsigned long long)p;
  u32x4 r;
  r[0] = __builtin_amdgcn_readfirstlane((unsigned)a);
  r[1] = __builtin_amdgcn_readfirstlane((unsigned)(a >> 32));
  r[2] = bytes;
  r[3] = 0x00020000u;
  return r;
}
__device__ __forceinline__ void glds16(const u32x4& srd, unsigned lds_dst, unsigned voff, unsigned soff) {
  asm volatile("s_mov_b32 m0, %0\n\ts_nop 0\n\tbuffer_load_dwordx4 %1, %2, %3 offen lds" ::"s"(lds_dst), "v"(voff), "s"(srd), "s"(soff) : "memory");
}
template <int IMM, class T>
__device__ __forceinline__ void gld16(T& dst, const u32x4& srd, unsigned voff, unsigned soff) {
  asm volatile("buffer_load_dwordx4 %0, %1, %2, %3 offen offset:%4" : "=v"(dst) : "v"(voff), "s"(srd), "s"(soff), "i"(IMM) : "memory");
}
template <int IMM, class T>
__device__ __forceinline__ void gld8(T& dst, const u32x4& srd, unsigned voff, unsigned soff) {
  asm volatile("buffer_load_dwordx2 %0, %1, %2, %3 offen offset:%4" : "=v"(dst) : "v"(voff), "s"(srd), "s"(soff), "i"(IMM) : "memory");
}
template <int IMM>
__device__ __forceinline__ void xst16(const f32x4& v, const u32x4& srd, unsigned voff, unsigned soff) {
  asm volatile("buffer_store_dwordx4 %0, %1, %2, %3 offen offset:%4 sc1" ::"a"(v), "v"(voff), "s"(srd), "s"(soff), "i"(IMM) : "memory");
}
template <int IMM>
__device__ __forceinline__ void xld16(f32x4& v, const u32x4& srd, unsigned voff, unsigned soff) {
  asm volatile("buffer_load_dwordx4 %0, %1, %2, %3 offen offset:%4 sc1" : "=v"(v) : "v"(voff), "s"(srd), "s"(soff), "i"(IMM) : "memory");
}
__device__ __forceinline__ void gld4(float& dst, const u32x4& srd, unsigned voff, unsigned soff) {
  asm volatile("buffer_load_dword %0, %1, %2, %3 offen" : "=v"(dst) : "v"(voff), "s"(srd), "s"(soff) : "memory");
}
__device__ __forceinline__ void glds4(const u32x4& srd, unsigned lds_dst, unsigned voff, unsigned soff) {
  asm volatile("s_mov_b32 m0, %0\n\ts_nop 0\n\tbuffer_load_dword %1, %2, %3 offen lds" ::"s"(lds_dst), "v"(voff), "s"(srd), "s"(soff) : "memory");
}
__device__ __forceinline__ void sld8(unsigned long long& sink, unsigned long long base, unsigned off) {
  asm volatile("s_load_dwordx2 %0, %1, %2" : "+s"(sink) : "s"(base), "s"(off) : "memory");
}
__device__ __forceinline__ bf16x8 tr_frag(LAS char* p0, LAS char* p1) {
  const bf16x4 lo = __builtin_amdgcn_ds_read_tr16_b64_v4bf16((LAS bf16x4*)p0);
  const bf16x4 hi = __builtin_amdgcn_ds_read_tr16_b64_v4bf16((LAS bf16x4*)p1);
  return __builtin_shufflevector(lo, hi, 0, 1, 2, 3, 4, 5, 6, 7);
}
template <bool WT> __device__ __forceinline__ void gst16(void* p, const u32x4& v) {
  if (WT) asm volatile("global_store_dwordx4 %0, %1, off sc1\n\ts_nop 1" ::"v"(p), "v"(v) : "memory");
  else *(u32x4*)p = v;
}
template <bool WT> __device__ __forceinline__ void gst8(void* p, const u32x2& v) {
  if (WT) asm volatile("global_store_dwordx2 %0, %1, off sc1\n\ts_nop 1" ::"v"(p), "v"(v) : "memory");
  else *(u32x2*)p = v;
}
#define MFMA(a, b, c) __builtin_amdgcn_mfma_f32_16x16x32_bf16(a, b, c, 0, 0, 0)
__device__ __forceinline__ void mfma_acc(f32x4& c, const bf16x8& a, const bf16x8& b) {
  asm volatile("v_mfma_f32_16x16x32_bf16 %0, %1, %2, %0" : "+a"(c) : "v"(a), "v"(b));
}
__device__ __forceinline__ void mfma_acc_va(f32x4& c, const bf16x8& a, const bf16x8& b) {
  asm volatile("v_mfma_f32_16x16x32_bf16 %0, %1, %2, %0" : "+a"(c) : "v"(a), "a"(b));
}
__device__ __forceinline__ void mfma_acc_av(f32x4& c, const bf16x8& a, const bf16x8& b) {
  asm volatile("v_mfma_f32_16x16x32_bf16 %0, %1, %2, %0" : "+a"(c) : "a"(a), "v"(b));
}
template <bool AG0, bool AG1, bool Z>
__device__ __forceinline__ void mfma_x(f32x4& c, const bf16x8& a, const bf16x8& b) {
  if constexpr (!Z) {
    if constexpr (AG0) mfma_acc_av(c, a, b); else if constexpr (AG1) mfma_acc_va(c, a, b); else mfma_acc(c, a, b);
  } else if constexpr (AG0) asm volatile("v_mfma_f32_16x16x32_bf16 %0, %1, %2, 0" : "=&a"(c) : "a"(a), "v"(b));
  else if constexpr (AG1) asm volatile("v_mfma_f32_16x16x32_bf16 %0, %1, %2, 0" : "=&a"(c) : "v"(a), "a"(b));
  else asm volatile("v_mfma_f32_16x16x32_bf16 %0, %1, %2, 0" : "=&a"(c) : "v"(a), "v"(b));
}
__device__ __forceinline__ void gld16a(bf16x8& dst, const u32x4& srd, unsigned voff, unsigned soff) {
  asm volatile("buffer_load_dwordx4 %0, %1, %2, %3 offen" : "=a"(dst) : "v"(voff), "s"(srd), "s"(soff) : "memory");
}
template <bool AG> __device__ __forceinline__ void PINR(bf16x8& x) {
  if (AG) asm volatile("" : "+a"(x)); else asm volatile("" : "+v"(x));
}
__device__ __forceinline__ void PINA(f32x4& c) { asm volatile("" : "+a"(c)); }

template <int MF, int KS, int BI, int ST, int AD, int XR, int PRO, int XE, bool LW>
struct Sched {
  static constexpr int NS = 4 * AD, BP = (BI + KS - 1) / KS, MAXX = 64;
  int total[NS + 1] = {};
  int posA[NS + AD + 1] = {}, posB[NS + ST + 2] = {};
  int nx[AD] = {};
  int xkind[AD][MAXX] = {}, xidx[AD][MAXX] = {}, xpc[AD][MAXX] = {};
  int npro = 0, pkind[MAXX] = {}, pidx[MAXX] = {}, ppc[MAXX] = {};
  constexpr Sched() {
    int n = 0;
    int qk[512] = {}, qi[512] = {}, qp[512] = {}, nq = 0, hq = 0;
    for (int p = 0; p < BI; ++p) { qk[nq] = 1; qi[nq] = 0; qp[nq] = p; ++nq; }
    for (int a = 0; a < AD || (a + 1) / KS < ST; ++a) {
      if (!LW && (a + 1) % KS == 0 && (a + 1) / KS < ST) for (int p = 0; p < BI; ++p) { qk[nq] = 1; qi[nq] = (a + 1) / KS; qp[nq] = p; ++nq; }
      if (a < AD) for (int m = 0; m < MF; ++m) { qk[nq] = 0; qi[nq] = a; qp[nq] = m; ++nq; }
    }
    while (hq < nq && hq < PRO) {
      pkind[hq] = qk[hq]; pidx[hq] = qi[hq]; ppc[hq] = qp[hq];
      ++n; if (qk[hq]) posB[qi[hq]] = n; else posA[qi[hq]] = n;
      ++hq;
    }
    npro = hq;
    for (int j = 0; j < NS; ++j) {
      total[j] = n;
      if (j < AD) {
        int c = 0;
        while (hq < nq && (c < XR || j == AD - 1)) {
          xkind[j][c] = qk[hq]; xidx[j][c] = qi[hq]; xpc[j][c] = qp[hq];
          ++n; if (qk[hq]) posB[qi[hq]] = n; else posA[qi[hq]] = n;
          ++hq; ++c;
        }
        nx[j] = c;
      }
      if (XE && j >= AD && j % XE == 0) ++n;
      if (j >= 1) { n += MF; posA[j - 1 + AD] = n; }
      if (!LW && j + 1 >= KS) {
        const int g = (j + 1) % KS, T = (j + 1) / KS - 1 + ST;
        for (int p = g * BP; p < (g + 1) * BP && p < BI; ++p) { ++n; posB[T] = n; }
      }
    }
    total[NS] = n;
  }
  constexpr int clampv(int v) const { return v > 63 ? 63 : v; }
  constexpr int waitA(int j) const { return clampv(total[j] - posA[j]); }
  constexpr int waitB(int j) const { return LW ? 63 : clampv(total[j] - posB[j / KS + 1]); }
  constexpr int min3(int a, int b, int c) const { return a < b ? (a < c ? a : c) : (b < c ? b : c); }
  constexpr int loopA(int r) const { return min3(waitA(AD + r), waitA(2 * AD + r), waitA(3 * AD + r)); }
  constexpr int loopB(int r) const { return min3(waitB(AD + r), waitB(2 * AD + r), waitB(3 * AD + r)); }
  constexpr bool ok() const {
    for (int j = 0; j < NS; ++j) {
      if (posA[j] <= 0 || posA[j] > total[j]) return false;
      if (!LW && j % KS == KS - 1 && (posB[j / KS + 1] <= 0 || posB[j / KS + 1] > total[j])) return false;
    }
    for (int j = 0; j < AD; ++j) if (nx[j] > MAXX) return false;
    if (posB[0] > total[0] || PRO > MAXX || 3 * AD / KS + 1 < ST) return false;
    return true;
  }
};

template <int MODE_, int MF_, int NF_, int KS_, int ST_, int AD_, int XR_, int PRO_, int KTOT_, int KLD_, int XE_, int MU_ = 0, int LW_ = 0, int SPF_ = 0>
struct Cfg {
  static constexpr int MODE = MODE_, MF = MF_, NF = NF_, KS = KS_, ST = ST_, AD = AD_, XR = XR_, PRO = PRO_, KTOT = KTOT_, KLD = KLD_, XE = XE_, NFX = NF_ + MU_;
  static constexpr int LW = LW_;
  static constexpr int SPF = SPF_, NT = SPF_ ? 512 : (LW_ ? 320 : 256);
  static constexpr int BK = 32 * KS;
  static constexpr int ROWB = (MODE == 3) ? 224 : NF * 32;
  static constexpr int SUB = (MODE == 3) ? BK * ROWB : 0;
  static constexpr int STAGE = BK * NF * 32;
  static constexpr int BI = STAGE / 4096;
  static constexpr int LDS_BYTES = ST * STAGE;
  static_assert((XE == 0 || AD % XE == 0) && STAGE % 4096 == 0 && AD % KS == 0 && AD % 2 == 0 && (KTOT / 32) % AD == 0, "");
};

template <class C> struct SchedOf {
  using SC = Sched<C::MF, C::KS, C::BI, C::ST, C::AD, C::XR, C::PRO, C::XE, (C::LW > 0)>;
  static constexpr SC S{};
};
template <class C> __device__ __forceinline__ int b_swz(int row) {
  if (C::MODE == 3) return 0;
  if (C::MODE == 1) return ((row >> 1) & 1) | (((row >> 3) & 1) << 1);
  if (C::ROWB % 256 == 0) return row & 7;
  return (row >> 1) & 3;
}

struct NoExt { template <class... T> __device__ __forceinline__ void operator()(T&&...) const {} };
template <class C, int NEPI, class Mid, class Ext>
__device__ __forceinline__ bool gemm_main(f32x4 (&acc)[C::MF][C::NFX], LAS char* lds, const u16* A, const u16* B0, const u16* B1, unsigned b_bytes,
                                          int ldb, const u32x4& swarm, const u32x4& spf, Mid&& mid, Ext&& ext TSARG, int cswap = 0) {
  constexpr int MF = C::MF, NF = C::NFX, KS = C::KS, ST = C::ST, AD = C::AD, BI = C::BI, ROWB = C::ROWB, NMM = MF * NF;
  using SO = SchedOf<C>;
  static_assert(SO::S.ok(), "schedule");
  constexpr int BP = SO::SC::BP;
  TSTAMP(0);
  const int lane = threadIdx.x & 63;
  const int wave = __builtin_amdgcn_readfirstlane(threadIdx.x >> 6);
  const unsigned lds0 = (unsigned)(size_t)lds;
  const u32x4 sa = mk_srd(A + (size_t)wave * MF * 16 * C::KLD, (unsigned)((MF - 1) * 16 * C::KLD + 16 * C::KTOT) * 2);
  const u32x4 sb = mk_srd((C::MODE == 3 && wave >= 2) ? B1 : B0, b_bytes);
  unsigned a_off[MF];
#pragma unroll
  for (int m = 0; m < MF; ++m) a_off[m] = lane * 16 + m * (C::KLD * 32);
  const unsigned b_tile = (unsigned)(C::BK * ldb * 2);
  auto src_of = [&](int piece) -> unsigned {
    if (C::MODE == 3) {
      const int ch = (piece % (2 * C::BI)) * 64 + lane, row = ch / 14, c = ch % 14;
      return (unsigned)(row * ldb * 2 + c * 16);
    }
    constexpr int CPR = ROWB / 16;
    const int ch = piece * 64 + lane, row = ch / CPR, cs = ch % CPR, c = cs ^ (b_swz<C>(row) << 1);
    const int col = (C::MODE == 1) ? (c >> 3) * 1024 + (c & 7) * 8 : (c ^ cswap) * 8;
    return (unsigned)(row * ldb * 2 + col * 2);
  };
  if constexpr (C::SPF > 0) {
    if (wave > 4) {
      const unsigned long long base = ((unsigned long long)spf[1] << 32) | spf[0];
      unsigned long long sink = 0;
      unsigned off = (wave - 5) * 128 + 60;
      const unsigned last = spf[2] - 68;
      auto burst = [&](auto NC) {
#pragma unroll
        for (int i = 0; i < decltype(NC)::value; ++i) sld8(sink, base, off + i * 384 < last ? off + i * 384 : last);
        off += decltype(NC)::value * 384;
      };
      burst(std::integral_constant<int, SPB>{});
      BAR();
#pragma unroll 1
      for (int t = 0; t < C::KTOT / C::BK; ++t) {
        if (off <= last) burst(std::integral_constant<int, C::SPF>{});
        BAR();
      }
      if (off <= last) burst(std::integral_constant<int, SPE>{});
      sfor<NEPI>([&](auto) { BAR(); });
      asm volatile("s_waitcnt lgkmcnt(0)" : "+s"(sink)::"memory");
      return false;
    }
  }
  if constexpr (C::LW > 0) {
    if (wave == 4) {
      constexpr int NP = 4 * C::BI, INF = C::LW;
      static_assert(INF >= 2 && INF < ST && INF * NP <= 63, "");
      unsigned src[NP];
#pragma unroll
      for (int i = 0; i < NP; ++i) src[i] = src_of(i);
      auto tile = [&](int slot, unsigned t) {
        sfor<NP>([&](auto i) { glds16(sb, lds0 + slot * C::STAGE + decltype(i)::value * 1024, src[decltype(i)::value], t * b_tile); });
      };
      tile(1, 1);
      BAR();
      sfor<INF - 2>([&](auto t) { tile(decltype(t)::value + 2, decltype(t)::value + 2); });
      int slot = INF;
      unsigned long long sink = 0;
#pragma unroll 1
      for (int t = 0; t < C::KTOT / C::BK; ++t) {
        tile(slot, t + INF);
        slot = (slot + 1 == ST) ? 0 : slot + 1;
        if constexpr (C::SPF > 0) {
          constexpr unsigned o0 = 3 * (SPB + C::KTOT / C::BK * C::SPF + SPE) * 128 + 60;
#pragma unroll
          for (int i = 0; i < SPL; ++i) {
            const unsigned o = o0 + (t * SPL + i) * 128;
            if (o + 8 <= spf[2]) sld8(sink, ((unsigned long long)spf[1] << 32) | spf[0], o);
          }
        }
        WAIT_V((INF - 1) * NP);
        BAR();
      }
      WAIT_V(0);
      sfor<NEPI>([&](auto) { BAR(); });
      asm volatile("s_waitcnt lgkmcnt(0)" : "+s"(sink)::"memory");
      return false;
    }
  }
  unsigned b_src[BI];
#pragma unroll
  for (int i = 0; i < BI; ++i) b_src[i] = src_of(wave * BI + i);
  const unsigned b_dst = lds0 + wave * BI * 1024;

  const int G = lane >> 4, kr = (lane & 15) >> 2, p = lane & 3;
  const int r0 = (C::MODE == 1 ? 8 : 4) * G + kr;
  constexpr int RD2 = (C::MODE == 1 ? 4 : 16) * ROWB;
  constexpr int NV = (C::MODE == 3) ? 1 : (ROWB % 256 == 0 ? 8 : 4);
  int b_rd[NV];
#pragma unroll
  for (int v = 0; v < NV; ++v) b_rd[v] = r0 * ROWB + ((v ^ b_swz<C>(r0)) << 5) + p * 8;
  LAS char* xfr = lds + C::LDS_BYTES + (lane & 1) * 64 + G * 16;
  auto half = [&](LAS char* base, int n, int s, int hf, int step) -> bf16x4 {
    if (n == C::NF) return *(const LAS bf16x4*)(xfr + step * 128 + hf * 8);
    int off;
    if (C::MODE == 3) off = b_rd[0] + (n & 1) * C::SUB + (n >> 1) * 32;
    else off = b_rd[n % NV] + (n / NV) * (NV * 32);
    off += s * 32 * ROWB + hf * RD2;
    return __builtin_amdgcn_ds_read_tr16_b64_v4bf16((LAS bf16x4*)(base + off));
  };

  bf16x8 ar[AD][MF];
  bf16x4 bf[2][NF][2];
  auto issueA = [&](auto RC, auto MC, unsigned step) {
    const unsigned so = step < C::KTOT / 32 ? step * 1024 : 0x7ff00000u;
    if constexpr (C::LW > 0) gld16a(ar[decltype(RC)::value][decltype(MC)::value], sa, a_off[decltype(MC)::value], so);
    else gld16<0>(ar[decltype(RC)::value][decltype(MC)::value], sa, a_off[decltype(MC)::value], so);
  };
  if constexpr (C::NFX > C::NF) glds16(swarm, lds0 + C::LDS_BYTES + wave * 1024, lane * 16, wave * 1024);
  sfor<SO::S.npro>([&](auto LC) {
    constexpr int l = decltype(LC)::value, kind = SO::S.pkind[l], idx = SO::S.pidx[l], pc = SO::S.ppc[l];
    if constexpr (kind) glds16(sb, b_dst + idx * C::STAGE + pc * 1024, b_src[pc], idx * b_tile);
    else issueA(std::integral_constant<int, idx>{}, std::integral_constant<int, pc>{}, idx);
  });
#pragma unroll
  for (int m = 0; m < MF; ++m)
#pragma unroll
    for (int n = 0; n < NF; ++n) if (!ZC) { acc[m][n] = f32x4{0.f, 0.f, 0.f, 0.f}; PINA(acc[m][n]); }
  TSTAMP(1);
  { constexpr int w0 = SO::S.total[0] - SO::S.posB[0]; WAIT_V(w0); }
  BAR();
  TSTAMP(2);
#pragma unroll
  for (int n = 0; n < NF; ++n) { bf[0][n][0] = half(lds, n, 0, 0, 0); bf[0][n][1] = half(lds, n, 0, 1, 0); }
  SB();

  int slot = 0, fslot = 0;
  auto step = [&](auto JC, auto FC, int j0) {
    constexpr int R = decltype(JC)::value;
    constexpr bool FIRST = decltype(FC)::value;
    constexpr int s = R % KS, cur = R & 1, nxt = cur ^ 1;
    constexpr int WA = FIRST ? SO::S.waitA(R) : SO::S.loopA(R), WB = (R % KS != KS - 1) ? 63 : (FIRST ? SO::S.waitB(R) : SO::S.loopB(R));
    WAIT_V(WA);
    sfor<MF>([&](auto m) { PINR<(C::LW > 0)>(ar[R][decltype(m)::value]); });
    if constexpr (s == KS - 1) {
      WAIT_L0();
      if constexpr (WB < WA) WAIT_V(WB);
      BAR();
      fslot = slot;
      slot = (slot + 1 == ST) ? 0 : slot + 1;
    }
    LAS char* rbase = lds + slot * C::STAGE;
    constexpr int ns = (s + 1) % KS;
    constexpr bool HASB = !(FIRST && R + 1 < KS) && C::LW == 0;
    constexpr int g = (R + 1) % KS;
    constexpr int NB = HASB ? ((g + 1) * BP <= BI ? BP : (BI - g * BP > 0 ? BI - g * BP : 0)) : 0;
    constexpr int NX = FIRST ? SO::S.nx[R] : (C::XE && R % (C::XE ? C::XE : 1) == 0 ? 1 : 0);
    constexpr int NA = (FIRST && R == 0) ? 0 : MF, RP = (R + AD - 1) % AD;
    constexpr int NL = NX + NA + NB;
    const unsigned gdst = b_dst + fslot * C::STAGE;
    const unsigned gsoff = (unsigned)((j0 + R + 1) / KS - 1 + ST) * b_tile;
    SB();
    sfor<NMM>([&](auto IC) {
      constexpr int i = decltype(IC)::value, n = i / MF, m = i % MF;
      const bf16x8 bb = __builtin_shufflevector(bf[cur][n][0], bf[cur][n][1], 0, 1, 2, 3, 4, 5, 6, 7);
      constexpr bool Z = ZC && FIRST && R == 0;
      if constexpr (C::MODE == 1 && n >= 4) mfma_x<(C::LW > 0), false, Z>(acc[m][n], ar[R][m], bb);
      else mfma_x<false, (C::LW > 0), Z>(acc[m][n], bb, ar[R][m]);
      SB();
      constexpr int SPAN = (NMM * 7) / 8 >= 2 * NF ? (NMM * 7) / 8 : NMM;
      sfor<2 * NF>([&](auto NC) {
        constexpr int nn = decltype(NC)::value;
        if constexpr ((nn * SPAN) / (2 * NF) == i) bf[nxt][nn >> 1][nn & 1] = half(rbase, nn >> 1, ns, nn & 1, j0 + R + 1);
      });
      sfor<NL>([&](auto LC) {
        constexpr int l = decltype(LC)::value;
        if constexpr (((2 * l + 1) * NMM) / (2 * (NL ? NL : 1)) == i) {
          if constexpr (l < NX && !FIRST) {
            constexpr int XS = C::MODE == 1 ? XS1 : XS3, XT = C::MODE == 1 ? XT1 : XT3;
            glds4(swarm, lds0 + C::LDS_BYTES + wave * 256, lane * XS, j0 + R >= XT ? (unsigned)(j0 + R - XT) * (64 * XS / (C::XE ? C::XE : 1)) : 0x7ff00000u);
          } else if constexpr (l < NX) {
            constexpr int kind = SO::S.xkind[R][l], idx = SO::S.xidx[R][l], pc = SO::S.xpc[R][l];
            if constexpr (kind) glds16(sb, b_dst + idx * C::STAGE + pc * 1024, b_src[pc], idx * b_tile);
            else issueA(std::integral_constant<int, idx>{}, std::integral_constant<int, pc>{}, idx);
          } else if constexpr (l < NX + NA) {
            issueA(std::integral_constant<int, RP>{}, std::integral_constant<int, l - NX>{}, (unsigned)(j0 + R - 1 + AD));
          } else {
            constexpr int pc = g * BP + (l - NX - NA);
            glds16(sb, gdst + pc * 1024, b_src[pc], gsoff);
          }
        }
      });
      SB();
    });
  };
  sfor<AD>([&](auto r) { step(r, std::true_type{}, 0); });
  mid();
  SB();
#pragma unroll 1
  for (int j0 = AD; j0 < C::KTOT / 32; j0 += AD) sfor<AD>([&](auto r) { step(r, std::false_type{}, j0); });
  asm volatile("s_nop 15\n\ts_nop 15" ::: "memory");
#pragma unroll
  for (int m = 0; m < MF; ++m)
#pragma unroll
    for (int n = 0; n < NF; ++n) PINA(acc[m][n]);
  TSTAMP(3);
  ext();
  WAIT_V(0);
  sfor<AD>([&](auto r) { sfor<MF>([&](auto m) { PINR<(C::LW > 0)>(ar[decltype(r)::value][decltype(m)::value]); }); });
  WAIT_L0();
  TSTAMP(5);
  return true;
}
#define C1_SPF 6
#define C1_ST 4
#define C1_AD 4
#define C1_XR 6
#define C2_ST 4
#define C2_XR 3
#define C3_ST 4
#define C3_XR 10
#define C4_ST 4
#define C4_XR 4
#define C1_PRO 16
#define C2_PRO 16
#define C3_PRO 18
#define C4_PRO 16
#define MAP4 0
#define WTALL 1
constexpr bool WT0 = WTALL, WT1 = WTALL, WTO = WTALL, WT2 = WTALL, WT3 = true, WT4 = WTALL;
#define PFMODE 2
#define C1_XE (C1_SPF ? 0 : 4)
#define C3_XE 4
#define C1_LW 2
#define C2_LW 3
#define C4_LW 3
#define EPQ 2
#define PFR 704
#define C2_SPF 0
#define PFRO PFR
#define PFR2 PFRO
static_assert(PFR % 16 == 0 && PFRO % 16 == 0 && PFR2 % 16 == 0 && (C2_SPF == 0 || PFR2 > PFRO) && PFR <= PFRO && PFRO <= PFR2 && PFR2 <= 1024 && (PFRO - PFR) * 14 <= 8192 && (1024 - PFR2) * 7 <= 4096, "");
#define LNR 1024
#define ALIAS 2
#define CPF 16
#define G1SYM "_Z12gemm1_kernelPKtS0_PtPfS0_S0_"

__device__ __forceinline__ float phi(float t) {
  const float e = __builtin_amdgcn_fmed3f(__builtin_amdgcn_exp2f(t * 1.4426950408889634f), 0.f, 1.f);
  return (fmaxf(t, 0.f) + e) + 1e-6f;
}
__device__ __forceinline__ bf16x8 pack8(const f32x4& a, const f32x4& b) {
  u32x4 o = {pack_bf16(a[0], a[1]), pack_bf16(a[2], a[3]), pack_bf16(b[0], b[1]), pack_bf16(b[2], b[3])};
  return __builtin_bit_cast(bf16x8, o);
}
__device__ __forceinline__ f32x4 phi4(const f32x4& a) { return f32x4{phi(a[0]), phi(a[1]), phi(a[2]), phi(a[3])}; }

constexpr int KVP = 1024 + 32;
using C1 = Cfg<1, 2, 12, 2, C1_ST, C1_AD, C1_XR, C1_PRO, 1024, 1024, C1_XE, 0, C1_LW, C1_SPF>;
constexpr int LDS1 = C1::LDS_BYTES + 1024 + 48 * 1024;
__global__ __launch_bounds__(C1::NT, 1) void gemm1_kernel(const u16* __restrict__ xn, const u16* __restrict__ wqkv, u16* __restrict__ pq,
                                                       float* __restrict__ kvp, const u16* __restrict__ wg,
                                                       const u16* __restrict__ wu) {
  extern __shared__ __attribute__((aligned(16))) char smem[];
  LAS char* lds = (LAS char*)smem;
  const int b = blockIdx.x, xcd = b & 7, idx = b >> 3;
  const int tm = (xcd >> 2) * 8 + (idx & 7), tn = (xcd & 3) * 4 + (idx >> 3);
  f32x4 acc[2][12];
  TSDECL;
  const int lane = threadIdx.x & 63, wave = __builtin_amdgcn_readfirstlane(threadIdx.x >> 6), gw = b * 4 + wave;
  const u32x4 sw = mk_srd(b < 128 ? wg + b * (PFR * 28) : wu + (b - 128) * (PFR * 28), PFR * 56);
  if (!gemm_main<C1, 1>(acc, lds, xn + (size_t)tm * 128 * D_, wqkv + tn * 64, wqkv, (unsigned)(D_ * 3 * D_ - tn * 64) * 2, 3 * D_, sw, sw, [] {}, NoExt{} TSPASS)) return;
  LAS f32x4* red = (LAS f32x4*)(lds + C1::LDS_BYTES + 1024);
  const u32x4 one4 = {0x3f803f80u, 0x3f803f80u, 0x3f803f80u, 0x3f803f80u};
  const f32x4 z4 = {0.f, 0.f, 0.f, 0.f};
#pragma unroll
  for (int hh = 0; hh < 2; ++hh) {
    const bf16x8 v0 = pack8(acc[0][8 + 2 * hh], acc[1][8 + 2 * hh]), v1 = pack8(acc[0][9 + 2 * hh], acc[1][9 + 2 * hh]);
#pragma unroll
    for (int i = 0; i < 2; ++i) {
      const bf16x8 pk = pack8(phi4(acc[0][4 + 2 * hh + i]), phi4(acc[1][4 + 2 * hh + i]));
      red[((wave * 2 + hh) * 6 + 2 * i) * 64 + lane] = MFMA(pk, v0, z4);
      red[((wave * 2 + hh) * 6 + 2 * i + 1) * 64 + lane] = MFMA(pk, v1, z4);
      red[((wave * 2 + hh) * 6 + 4 + i) * 64 + lane] = MFMA(pk, __builtin_bit_cast(bf16x8, one4), z4);
    }
  }
  SB();
#pragma unroll
  for (int m = 0; m < 2; ++m)
#pragma unroll
    for (int hh = 0; hh < EPQ; ++hh)
      gst16<WT1>(pq + ((size_t)((tm * 8 + wave * 2 + m) * 32 + tn * 2 + hh) * 64 + lane) * 8, __builtin_bit_cast(u32x4, pack8(phi4(acc[m][2 * hh]), phi4(acc[m][2 * hh + 1]))));
  WAIT_L0();
  BAR();
#pragma unroll
  for (int e = 0; e < 3; ++e) {
    const int blk = e * 4 + wave, hh = blk / 6, bb = blk % 6;
    const f32x4 s = (red[blk * 64 + lane] + red[(12 + blk) * 64 + lane]) + (red[(24 + blk) * 64 + lane] + red[(36 + blk) * 64 + lane]);
    float* dst = kvp + ((size_t)(tn * 2 + hh) * 16 + tm) * KVP;
    if (bb < 4) gst16<WT1>(dst + bb * 256 + lane * 4, __builtin_bit_cast(u32x4, s));
    else if ((lane & 15) == 0) gst16<WT1>(dst + 1024 + (bb - 4) * 16 + (lane >> 4) * 4, __builtin_bit_cast(u32x4, s));
  }
  SB();
#pragma unroll
  for (int m = 0; m < 2; ++m)
#pragma unroll
    for (int hh = EPQ; hh < 2; ++hh)
      gst16<WT1>(pq + ((size_t)((tm * 8 + wave * 2 + m) * 32 + tn * 2 + hh) * 64 + lane) * 8, __builtin_bit_cast(u32x4, pack8(phi4(acc[m][2 * hh]), phi4(acc[m][2 * hh + 1]))));
  WAIT_V(0);
  TSTAMP(4);
  TSOUT(0, 6);
}

__global__ __launch_bounds__(256) void o_kernel(const u16* __restrict__ pq, const float* __restrict__ kvp, u16* __restrict__ o,
                                                const u16* __restrict__ wg, const u16* __restrict__ wu) {
  __shared__ __attribute__((aligned(16))) float sh[1024];
  const int t = threadIdx.x, lane = t & 63, wave = __builtin_amdgcn_readfirstlane(t >> 6);
  const int head = blockIdx.x & 31, rg = blockIdx.x >> 5;
  const u32x4 sk = mk_srd(kvp + (size_t)head * 16 * KVP, 16 * KVP * 4), sq = mk_srd(pq + ((size_t)(rg * 16 + wave * 4) * 32 + head) * 512, 100 * 1024);
  const int gw = blockIdx.x * 4 + wave;
  const u32x4 sw = mk_srd((gw < 512 ? wg : wu) + (size_t)PFR * F_ + (gw & 511) * ((PFRO - PFR) * 7), (PFRO - PFR) * 14);
  f32x4 pv[16], zp;
  bf16x8 qf[4];
  float pf[2];
  sfor<16>([&](auto i) { gld16<0>(pv[decltype(i)::value], sk, t * 16, decltype(i)::value * KVP * 4); });
  gld16<0>(zp, sk, t < 128 ? (t >> 3) * KVP * 4 + 4096 + (t & 7) * 16 : 0x7ffffff0u, 0);
  sfor<4>([&](auto m) { gld16<0>(qf[decltype(m)::value], sq, lane * 16, decltype(m)::value * 32768); });
  gld4(pf[0], sw, lane * 64, 0);
  gld4(pf[1], sw, lane * 64, 4096);
  WAIT_V(2);
  sfor<16>([&](auto i) { PIN(pv[decltype(i)::value]); });
  sfor<4>([&](auto m) { PIN(qf[decltype(m)::value]); });
  PIN(zp);
#pragma unroll
  for (int st = 8; st > 0; st >>= 1)
#pragma unroll
    for (int i = 0; i < st; ++i) pv[i] += pv[i + st];
  u32x2 pk2 = {pack_bf16(pv[0][0], pv[0][1]), pack_bf16(pv[0][2], pv[0][3])};
  *(u32x2*)((char*)sh + ((wave & 1) * 64 + lane) * 16 + (wave >> 1) * 8) = pk2;
  if (t < 128) *(f32x4*)(sh + 512 + t * 4) = zp;
  __syncthreads();
  const bf16x8 kv0 = *(const bf16x8*)((char*)sh + lane * 16), kv1 = *(const bf16x8*)((char*)sh + (64 + lane) * 16);
  f32x4 za = {0.f, 0.f, 0.f, 0.f}, zb = za;
#pragma unroll
  for (int i = 0; i < 16; ++i) {
    za += *(const f32x4*)(sh + 512 + i * 32 + (lane >> 4) * 4);
    zb += *(const f32x4*)(sh + 512 + i * 32 + 16 + (lane >> 4) * 4);
  }
  const bf16x8 zf = pack8(za, zb);
  const f32x4 z4 = {0.f, 0.f, 0.f, 0.f};
#pragma unroll
  for (int m = 0; m < 4; ++m) {
    const f32x4 n0 = MFMA(kv0, qf[m], z4), n1 = MFMA(kv1, qf[m], z4), dn = MFMA(zf, qf[m], z4);
    const float inv = 1.f / (dn[0] + 1e-6f);
    gst16<WTO>(o + ((size_t)((rg * 16 + wave * 4 + m) * 32 + head) * 64 + lane) * 8, __builtin_bit_cast(u32x4, pack8(n0 * inv, n1 * inv)));
  }
  WAIT_V(0);
  PIN(pf[0]); PIN(pf[1]);
}

using C2 = Cfg<2, 2, 4, 4, C2_ST, 8, C2_XR, C2_PRO, 1024, 1024, 0, 1, C2_LW, C2_SPF>;
constexpr int LDS2 = C2::LDS_BYTES + 4096 + 256;
__global__ __launch_bounds__(C2::NT, 1) void gemm2_kernel(const u16* __restrict__ o, const u16* __restrict__ wo, const u16* __restrict__ x,
                                                       u16* __restrict__ h, float* __restrict__ stats, const float* __restrict__ sumx,
                                                       const u16* __restrict__ rsw, float* __restrict__ mub,
                                                       const u16* __restrict__ wg, const u16* __restrict__ wu) {
  extern __shared__ __attribute__((aligned(16))) char smem[];
  LAS char* lds = (LAS char*)smem;
  const int b = blockIdx.x, xcd = b & 7, idx = b >> 3;
  const int tm = (xcd >> 1) * 4 + (idx & 3), tn = (xcd & 1) * 8 + (idx >> 2);
  const int lane = threadIdx.x & 63, wave = __builtin_amdgcn_readfirstlane(threadIdx.x >> 6);
  f32x4 acc[2][5];
  u32x2 res[2][4];
  float sxr[2];
  const u32x4 sx = mk_srd(x + (size_t)(tm * 128 + wave * 32) * D_ + tn * 64, 32 * D_ * 2);
  const u32x4 ssx = mk_srd(sumx + tm * 128 + wave * 32, 128), sr = mk_srd(rsw, 4096);
  const unsigned xo = (unsigned)((lane & 15) * D_ * 2 + (lane >> 4) * 8);
  const u32x4 sp = mk_srd((b < 128 ? wg : wu) + (size_t)PFRO * F_ + (b & 127) * ((PFR2 - PFRO) * 28), (PFR2 - PFRO) * 56);
  TSDECL;
  if (!gemm_main<C2, 0>(acc, lds, o + (size_t)tm * 128 * D_, wo + tn * 64, wo, (unsigned)(D_ * D_ - tn * 64) * 2, D_, sr, sp, [&] {
    sfor<2>([&](auto m) {
      sfor<4>([&](auto n) { gld8<decltype(n)::value * 32>(res[decltype(m)::value][decltype(n)::value], sx, xo, decltype(m)::value * 16 * D_ * 2); });
      gld4(sxr[decltype(m)::value], ssx, (lane & 15) * 4, decltype(m)::value * 64);
    });
  }, NoExt{} TSPASS)) return;
#pragma unroll
  for (int m = 0; m < 2; ++m) {
    unsigned w[8];
    PIN(sxr[m]);
    const float mu = (sxr[m] + (acc[m][4][0] + acc[m][4][1])) * (1.f / D_);
#pragma unroll
    for (int n = 0; n < 4; ++n) {
      PIN(res[m][n]);
      w[2 * n] = pack_bf16(bf_lo(res[m][n][0]) + acc[m][n][0] - mu, bf_hi(res[m][n][0]) + acc[m][n][1] - mu);
      w[2 * n + 1] = pack_bf16(bf_lo(res[m][n][1]) + acc[m][n][2] - mu, bf_hi(res[m][n][1]) + acc[m][n][3] - mu);
    }
    const int mb = tm * 8 + wave * 2 + m;
    gst16<WT2>(h + ((size_t)(mb * 32 + tn * 2) * 64 + lane) * 8, u32x4{w[0], w[1], w[2], w[3]});
    gst16<WT2>(h + ((size_t)(mb * 32 + tn * 2 + 1) * 64 + lane) * 8, u32x4{w[4], w[5], w[6], w[7]});
    float q = 0.f;
#pragma unroll
    for (int i = 0; i < 8; ++i) { const float a = bf_lo(w[i]), c = bf_hi(w[i]); q += a * a + c * c; }
    q += __shfl_xor(q, 16); q += __shfl_xor(q, 32);
    if (lane < 16) {
      stats[(size_t)(mb * 16 + lane) * 16 + tn] = q;
      if (tn == 0) mub[mb * 16 + lane] = mu;
    }
  }
  TSTAMP(4);
  TSOUT(1, 6);
}

using C3 = Cfg<3, 4, 14, 2, C3_ST, 4, C3_XR, C3_PRO, 1024, 1024, C3_XE>;
constexpr int LDS3 = C3::LDS_BYTES + 1024;
template <int PAR>
__device__ __forceinline__ void geglu_store(f32x4 (&acc)[4][14], const float (&var)[4], u16* g, int mb0, int tn, int lane) {
#pragma unroll
  for (int m = 0; m < 4; ++m) {
    const float ir2 = var[m], r = __builtin_amdgcn_rsqf(ir2), k1 = -2.f * 0.7978845608028654f * 1.4426950408889634f * r, k2 = k1 * 0.044715f * r * r;
    char* dst = (char*)g + ((size_t)(mb0 + m) * (F_ / 32) + ((tn * 7) >> 1)) * 1024;
    unsigned w[14];
#pragma unroll
    for (int n = 0; n < 7; ++n) {
      const f32x4 a = acc[m][2 * n], u = acc[m][2 * n + 1];
      const f32x4 arg = a * (a * a * k2 + k1);
      f32x4 d;
#pragma unroll
      for (int i = 0; i < 4; ++i) d[i] = __builtin_amdgcn_exp2f(arg[i]);
      d = d * ir2 + ir2;
#pragma unroll
      for (int i = 0; i < 4; ++i) d[i] = __builtin_amdgcn_rcpf(d[i]);
      const f32x4 o = a * u * d;
      w[2 * n] = pack_bf16(o[0], o[1]);
      w[2 * n + 1] = pack_bf16(o[2], o[3]);
    }
    if (PAR == 0) {
#pragma unroll
      for (int i = 0; i < 3; ++i) gst16<WT3>(dst + i * 1024 + lane * 16, u32x4{w[4 * i], w[4 * i + 1], w[4 * i + 2], w[4 * i + 3]});
      gst8<WT3>(dst + 3 * 1024 + lane * 16, u32x2{w[12], w[13]});
    } else {
      gst8<WT3>(dst + lane * 16 + 8, u32x2{w[0], w[1]});
#pragma unroll
      for (int i = 0; i < 3; ++i) gst16<WT3>(dst + (i + 1) * 1024 + lane * 16, u32x4{w[4 * i + 2], w[4 * i + 3], w[4 * i + 4], w[4 * i + 5]});
    }
  }
}
__global__ __launch_bounds__(256, 1) void gemm3_kernel(const u16* __restrict__ h, const u16* __restrict__ wg, const u16* __restrict__ wu,
                                                       const float* __restrict__ stats, u16* __restrict__ g,
                                                       const u16* __restrict__ wd) {
  extern __shared__ __attribute__((aligned(16))) char smem[];
  LAS char* lds = (LAS char*)smem;
  const int b = blockIdx.x, xcd = b & 7, idx = b >> 3;
  const int tm = idx & 7, tn = xcd * 4 + (idx >> 3);
  const int lane = threadIdx.x & 63, wave = __builtin_amdgcn_readfirstlane(threadIdx.x >> 6);
  f32x4 acc[4][14];
  f32x4 st[4];
  const u32x4 ss = mk_srd(stats + (size_t)(tm * 256 + wave * 64) * 16, 64 * 64);
  const u32x4 sw = mk_srd(wd + (b * 4 + wave) * 3584, 7168);
  TSDECL;
  gemm_main<C3, 0>(acc, lds, h + (size_t)tm * 256 * D_, wg + tn * 112, wu + tn * 112, (unsigned)(D_ * F_ - tn * 112) * 2, F_, sw, sw, [&] {
    sfor<4>([&](auto i) { gld16<decltype(i)::value * 16>(st[decltype(i)::value], ss, lane * 64, 0); });
    {
      constexpr int HB = (1024 - PFR2) * 7;
      const int gw = b * 4 + wave;
      const u32x4 s2 = mk_srd((gw < 512 ? wg : wu) + (size_t)PFR2 * F_ + (gw & 511) * (HB / 2), 512 * HB + HB);
      const unsigned vo = lane * 64 < HB ? lane * 64 : 0x7ff00000u;
      glds4(s2, (unsigned)(size_t)lds + C3::LDS_BYTES + wave * 256, vo, 0);
      glds4(s2, (unsigned)(size_t)lds + C3::LDS_BYTES + wave * 256, vo, 512 * HB);
    }
  }, [&] {
  float var[4];
  {
    sfor<4>([&](auto i) { PIN(st[decltype(i)::value]); });
    const f32x4 q4 = (st[0] + st[1]) + (st[2] + st[3]);
    const float v = ((q4[0] + q4[1]) + (q4[2] + q4[3])) * (1.f / D_) + 1e-5f;
#pragma unroll
    for (int m = 0; m < 4; ++m) var[m] = __shfl(v, m * 16 + (lane & 15));
  }
  TSTAMP(6);
  if (tn & 1) geglu_store<1>(acc, var, g, tm * 16 + wave * 4, tn, lane);
  else geglu_store<0>(acc, var, g, tm * 16 + wave * 4, tn, lane);
  } TSPASS);
  TSTAMP(4);
  TSOUT(2, 7);
}

using C4 = Cfg<2, 2, 8, 2, C4_ST, 8, C4_XR, C4_PRO, F_ / 2, F_, 0, 0, C4_LW>;
typedef __attribute__((address_space(1))) unsigned gu32;
__global__ __launch_bounds__(C4_LW ? 320 : 256, 1) void gemm4_kernel(const u16* __restrict__ g, const u16* __restrict__ wd, const u16* __restrict__ h,
                                                       const float* __restrict__ mub, u16* __restrict__ out, float* xbuf, unsigned* flags) {
  extern __shared__ __attribute__((aligned(16))) char smem[];
  LAS char* lds = (LAS char*)smem;
  const int b = blockIdx.x, xcd = b & 7, idx = b >> 3;
  const int kh = xcd & 1, tm = ((xcd >> 1) & 1) * 8 + (idx & 7), tn2 = (xcd >> 2) * 4 + (idx >> 3), tn = tn2 * 2 + kh;
  const int lane = threadIdx.x & 63, wave = __builtin_amdgcn_readfirstlane(threadIdx.x >> 6);
  f32x4 acc[2][8];
  u32x4 res[2][2];
  float mu[2];
  const u32x4 smu = mk_srd(mub + tm * 128 + wave * 32, 128);
  const u32x4 sh = mk_srd(h + ((size_t)(tm * 8 + wave * 2) * 32 + tn * 2) * 512, 34 * 1024);
  TSDECL;
  if (!gemm_main<C4, 1>(acc, lds, g + (size_t)tm * 128 * F_ + kh * (F_ / 2 * 16), wd + (size_t)kh * (F_ / 2) * D_ + tn2 * 128, wd,
                (unsigned)(F_ / 2 * D_ - tn2 * 128) * 2, D_, sh, sh, [&] {
    sfor<2>([&](auto m) {
      gld16<0>(res[decltype(m)::value][0], sh, lane * 16, decltype(m)::value * 32768);
      gld16<1024>(res[decltype(m)::value][1], sh, lane * 16, decltype(m)::value * 32768);
      gld4(mu[decltype(m)::value], smu, (lane & 15) * 4, decltype(m)::value * 64);
    });
  }, NoExt{} TSPASS, kh * 8)) return;
  const u32x4 so = mk_srd(xbuf + (size_t)(b * 4 + wave) * 2048, 8192), si = mk_srd(xbuf + (size_t)((b ^ 1) * 4 + wave) * 2048, 8192);
  sfor<8>([&](auto IC) {
    constexpr int i = decltype(IC)::value;
    xst16<(i & 3) * 1024>(acc[i >> 2][4 + (i & 3)], so, lane * 16, (i >> 2) * 4096);
  });
  WAIT_V(0);
  BAR();
  if (threadIdx.x == 0) __hip_atomic_store((gu32*)(flags + b * 32), 1u, __ATOMIC_RELAXED, __HIP_MEMORY_SCOPE_AGENT);
  TSTAMP(6);
  for (int spin = 0; spin < (1 << 22); ++spin) {
    if (__builtin_amdgcn_readfirstlane(__hip_atomic_load((gu32*)(flags + (b ^ 1) * 32), __ATOMIC_RELAXED, __HIP_MEMORY_SCOPE_AGENT))) break;
    __builtin_amdgcn_s_sleep(1);
  }
  TSTAMP(7);
  f32x4 oth[2][4];
  sfor<8>([&](auto IC) {
    constexpr int i = decltype(IC)::value;
    xld16<(i & 3) * 1024>(oth[i >> 2][i & 3], si, lane * 16, (i >> 2) * 4096);
  });
  LAS char* patch = lds + wave * (32 * 144);
  sfor<8>([&](auto IC) {
    constexpr int i = decltype(IC)::value, m = i >> 2, n = i & 3;
    PIN(res[m][n >> 1]); PIN(mu[m]);
    const unsigned r0 = res[m][n >> 1][(n & 1) * 2], r1 = res[m][n >> 1][(n & 1) * 2 + 1];
    const f32x4 base = f32x4{bf_lo(r0), bf_hi(r0), bf_lo(r1), bf_hi(r1)} + mu[m];
    WAIT_V(7 - i);
    PIN(oth[m][n]);
    const f32x4 v = base + (kh ? oth[m][n] + acc[m][n] : acc[m][n] + oth[m][n]);
    *(LAS u32x2*)(patch + (m * 16 + (lane & 15)) * 144 + n * 32 + (lane >> 4) * 8) = u32x2{pack_bf16(v[0], v[1]), pack_bf16(v[2], v[3])};
  });
#pragma unroll
  for (int i = 0; i < 4; ++i) {
    const int row = i * 8 + (lane >> 3);
    const u32x4 w = *(const LAS u32x4*)(patch + row * 144 + (lane & 7) * 16);
    gst16<WT4>(out + (size_t)(tm * 128 + wave * 32 + row) * D_ + tn * 64 + (lane & 7) * 8, w);
  }
  TSTAMP(4);
  TSOUT(3, 8);
}

__global__ __launch_bounds__(256) void ln1_kernel(const u16* __restrict__ x, u16* __restrict__ y, const u16* __restrict__ wqkv,
                                                  const u16* __restrict__ wo, float* __restrict__ sumx, u16* __restrict__ rsw,
                                                  unsigned* __restrict__ flags) {
  __shared__ float red[2][4][8];
  const int t = threadIdx.x, wave = t >> 6, r = t & 7, c0 = t >> 3, row = blockIdx.x * 8 + r;
  u32x4 v[4], pf[6];
  const u32x4 sx = mk_srd(x + (size_t)blockIdx.x * 8 * D_, 8 * D_ * 2);
  sfor<4>([&](auto i) { gld16<decltype(i)::value * 512>(v[decltype(i)::value], sx, r * D_ * 2 + c0 * 16, 0); });
  const u32x4 s1 = mk_srd(wqkv + (size_t)blockIdx.x * (LNR * 12), LNR * 24);
  sfor<6>([&](auto i) { gld16<0>(pf[decltype(i)::value], s1, t * 16, decltype(i)::value * 4096); });
  float cw;
  {
    unsigned long long pc;
    int delta;
    asm volatile("s_getpc_b64 %0\n\ts_mov_b32 %1, " G1SYM "@rel32@lo+4" : "=&s"(pc), "=s"(delta));
    const u32x4 sc = mk_srd((const char*)(pc + (long long)delta), delta < 0 ? (unsigned)(-delta) : 0u);
    gld4(cw, sc, ((blockIdx.x >> 3) * 256 + t) * CPF, 0);
  }
  u32x4 wr[2];
  const u32x4 s2 = mk_srd(wo + (size_t)blockIdx.x * 4096, 8192);
  gld16<0>(wr[0], s2, t * 32, 0);
  gld16<16>(wr[1], s2, t * 32, 0);
  WAIT_V(CPF ? 9 : 8);
  sfor<4>([&](auto i) { PIN(v[decltype(i)::value]); });
  float s = 0.f;
#pragma unroll
  for (int i = 0; i < 4; ++i)
#pragma unroll
    for (int j = 0; j < 4; ++j) s += bf_lo(v[i][j]) + bf_hi(v[i][j]);
  s += __shfl_xor(s, 8); s += __shfl_xor(s, 16); s += __shfl_xor(s, 32);
  if ((t & 63) < 8) red[0][wave][r] = s;
  __syncthreads();
  const float sx4 = red[0][0][r] + red[0][1][r] + red[0][2][r] + red[0][3][r];
  const float m = sx4 * (1.f / D_);
  if (t < 8) sumx[row] = sx4;
  if (t == 8) flags[blockIdx.x * 32] = 0u;
  float q = 0.f;
#pragma unroll
  for (int i = 0; i < 4; ++i)
#pragma unroll
    for (int j = 0; j < 4; ++j) { const float a = bf_lo(v[i][j]) - m, b = bf_hi(v[i][j]) - m; q += a * a + b * b; }
  q += __shfl_xor(q, 8); q += __shfl_xor(q, 16); q += __shfl_xor(q, 32);
  if ((t & 63) < 8) red[1][wave][r] = q;
  __syncthreads();
  const float var = (red[1][0][r] + red[1][1][r] + red[1][2][r] + red[1][3][r]) * (1.f / D_);
  const float rs = 1.f / sqrtf(var + 1e-5f);
#pragma unroll
  for (int i = 0; i < 4; ++i) {
    u32x4 o;
#pragma unroll
    for (int j = 0; j < 4; ++j) o[j] = pack_bf16((bf_lo(v[i][j]) - m) * rs, (bf_hi(v[i][j]) - m) * rs);
    const int c = c0 + 32 * i;
    gst16<WT0>(y + ((size_t)((row >> 4) * 32 + (c >> 2)) * 64 + (c & 3) * 16 + (row & 15)) * 8, o);
  }
  WAIT_V(0);
  sfor<6>([&](auto i) { PIN(pf[decltype(i)::value]); });
  PIN(wr[0]); PIN(wr[1]);
  PIN(cw);
  float ws = 0.f;
#pragma unroll
  for (int j = 0; j < 4; ++j) ws += (bf_lo(wr[0][j]) + bf_hi(wr[0][j])) + (bf_lo(wr[1][j]) + bf_hi(wr[1][j]));
#pragma unroll
  for (int d = 1; d < 64; d <<= 1) ws += __shfl_xor(ws, d);
  if ((t & 63) == 0) {
    const int k = blockIdx.x * 4 + wave, kk = k & 31;
    const unsigned hi = pack_bf16(ws, 0.f), lo = pack_bf16(ws - bf_lo(hi), 0.f);
    u16* dst = rsw + (k >> 5) * 64 + ((kk & 15) >> 2) * 8 + (kk & 3) + (kk >> 4) * 4;
    dst[0] = (u16)hi;
    dst[32] = (u16)lo;
  }
}

extern "C" void kernel_launch(void* const* d_in, const int* in_sizes, int n_in, void* d_out, int out_size, void* d_ws, size_t ws_size,
                              hipStream_t stream) {
  const u16* x = (const u16*)d_in[0];
  const u16* wqkv = (const u16*)d_in[1];
  const u16* wo = (const u16*)d_in[2];
  const u16* wg = (const u16*)d_in[4];
  const u16* wu = (const u16*)d_in[5];
  const u16* wd = (const u16*)d_in[6];
  u16* out = (u16*)d_out;
  char* ws = (char*)d_ws;
  const size_t MB = 1 << 20;
  u16* xn = (u16*)(ws);
  u16* pq = (u16*)(ws + 4 * MB);
  u16* o = (u16*)(ws + 8 * MB);
  u16* hbuf = (u16*)(ws + 16 * MB);
  float* kvp = (float*)(ws + 12 * MB);
  float* stats = (float*)(ws + 20 * MB);
  float* sumx = (float*)(ws + 21 * MB);
  float* mub = (float*)(ws + 21 * MB + 65536);
  u16* rsw = (u16*)(ws + 21 * MB + 131072);
  u16* g = (u16*)(ws + (ALIAS == 2 ? 0 : 24) * MB);
  unsigned* flags = (unsigned*)(ws + 22 * MB);
  float* xbuf = (float*)(ws + (ALIAS == 1 ? 0 : 40) * MB);

  (void)hipFuncSetAttribute((const void*)gemm1_kernel, hipFuncAttributeMaxDynamicSharedMemorySize, LDS1);
  (void)hipFuncSetAttribute((const void*)gemm2_kernel, hipFuncAttributeMaxDynamicSharedMemorySize, LDS2);
  (void)hipFuncSetAttribute((const void*)gemm3_kernel, hipFuncAttributeMaxDynamicSharedMemorySize, LDS3);
  (void)hipFuncSetAttribute((const void*)gemm4_kernel, hipFuncAttributeMaxDynamicSharedMemorySize, C4::LDS_BYTES);

  ln1_kernel<<<256, 256, 0, stream>>>(x, xn, wqkv, wo, sumx, rsw, flags);
  gemm1_kernel<<<256, C1::NT, LDS1, stream>>>(xn, wqkv, pq, kvp, wg, wu);
  o_kernel<<<256, 256, 0, stream>>>(pq, kvp, o, wg, wu);
  gemm2_kernel<<<256, C2::NT, LDS2, stream>>>(o, wo, x, hbuf, stats, sumx, rsw, mub, wg, wu);
  gemm3_kernel<<<256, 256, LDS3, stream>>>(hbuf, wg, wu, stats, g, wd);
  gemm4_kernel<<<256, C4_LW ? 320 : 256, C4::LDS_BYTES, stream>>>(g, wd, hbuf, mub, out, xbuf, flags);
}
```
